```python
import torch

def _softmax(x, axis=-1):
    return torch.softmax(x, dim=axis)
def _gelu(x):
    return torch.nn.functional.gelu(x, approximate="tanh")
def _silu(x):
    return torch.nn.functional.silu(x)
def _sigmoid(x):
    return torch.sigmoid(x)
def _layernorm(x, axis=-1, eps=1e-5):
    m = x.mean(dim=axis, keepdim=True)
    v = x.var(dim=axis, keepdim=True, unbiased=False)
    return (x - m) / torch.sqrt(v + eps)
def _scatter_add(tgt, idx, src, axis=0):
    return tgt.clone().index_add_(axis, idx, src)
def _segment_sum(src, seg, n):
    out = torch.zeros((n,) + src.shape[1:], dtype=src.dtype)
    return out.index_add_(0, seg, src)
def _pad(x, pw):
    flat = tuple(v for pair in reversed(pw) for v in pair)
    return torch.nn.functional.pad(x, flat)

kernel_name = 'concat_heads__softmax_full_mha__linear_attn_block__prompt'

def setup_inputs():

    _w = (lambda a: (lambda t: t.to(torch.bfloat16) if t.is_floating_point() else t)(torch.as_tensor(a)))
    def _A_setup():
        B, S, D = 1, 2048, 512
        return {
            "x": _w((lambda *s: torch.randn(*s, dtype=torch.bfloat16) * 0.2)(B, S, D)),
            "wqkv": _w((lambda *s: torch.randn(*s, dtype=torch.bfloat16) * 0.2)(D, 3 * D)),
            "wo": _w((lambda *s: torch.randn(*s, dtype=torch.bfloat16) * 0.05)(D, D)),
        }
    _A_in = _A_setup()

    def _B_setup():
        B, S, D = 1, 2048, 512
        return {
            "x": _w((lambda *s: torch.randn(*s, dtype=torch.bfloat16) * 0.05)(B, S, D)),
            "wqkv": _w((lambda *s: torch.randn(*s, dtype=torch.bfloat16) * 1.6)(D, 3 * D)),
            "wo": _w((lambda *s: torch.randn(*s, dtype=torch.bfloat16) * 0.05)(D, D)),
        }
    _B_in = _B_setup()

    return {
        "A_x": _A_in["x"],
        "A_wqkv": _A_in["wqkv"],
        "A_wo": _A_in["wo"],
        "B_x": _B_in["x"],
        "B_wqkv": _B_in["wqkv"],
        "B_wo": _B_in["wo"],
    }

def reference(A_x, A_wqkv, A_wo, B_x, B_wqkv, B_wo):
    def _A_ref(x, wqkv, wo):
        B, S, D = x.shape
        H = 32; Dh = D // H
        qkv = (lambda x, *d: x.permute(*d))((x @ wqkv).reshape(B, S, 3, H, Dh), 2, 0, 3, 1, 4)
        q, k, v = qkv[0], qkv[1], qkv[2]
        scale = Dh ** -0.5
        scores = torch.einsum("bhqd,bhkd->bhqk", q, k) * scale
        p = _softmax(scores, axis=-1)
        o = torch.einsum("bhqk,bhkd->bhqd", p, v)
        return (lambda x, *d: x.permute(*d))(o, 0, 2, 1, 3).reshape(B, S, D) @ wo

    def _B_ref(x, wqkv, wo):
        B, S, D = x.shape
        H = 32; Dh = D // H
        qkv = (lambda x, *d: x.permute(*d))((x @ wqkv).reshape(B, S, 3, H, Dh), 2, 0, 3, 1, 4)
        q, k, v = qkv[0], qkv[1], qkv[2]
        phi = lambda t: torch.where(t > 0, t + 1, torch.exp(t)) + 1e-6
        pq, pk = phi(q), phi(k)
        kv = torch.einsum("bhsd,bhse->bhde", pk, v)
        z = torch.einsum("bhsd->bhd", pk)
        num = torch.einsum("bhqd,bhde->bhqe", pq, kv)
        den = torch.einsum("bhqd,bhd->bhq", pq, z)[..., None] + 1e-6
        o = num / den
        return (lambda x, *d: x.permute(*d))(o, 0, 2, 1, 3).reshape(B, S, D) @ wo

    a = _A_ref(A_x, A_wqkv, A_wo)
    b = _B_ref(B_x, B_wqkv, B_wo)
    return torch.concatenate([a, b], axis=-1)
```

```cpp
#include <hip/hip_runtime.h>
#include <hip/hip_bf16.h>
#include <stdint.h>

typedef __attribute__((ext_vector_type(8))) __bf16 bf16x8;
typedef __attribute__((ext_vector_type(16))) __bf16 bf16x16;
typedef __attribute__((ext_vector_type(2))) __bf16 bf16x2;
typedef __attribute__((ext_vector_type(16))) float f32x16;
typedef __attribute__((ext_vector_type(8))) float f32x8;
typedef __attribute__((ext_vector_type(4))) float f32x4;
typedef __attribute__((ext_vector_type(2))) float f32x2;
typedef __attribute__((ext_vector_type(4))) unsigned u32x4;
typedef __attribute__((ext_vector_type(32))) unsigned u32x32;
typedef unsigned long long u64;
#define LAS __attribute__((address_space(3)))
#define GAS __attribute__((address_space(1)))
#define DI __device__ __forceinline__

constexpr float LOG2E = 1.4426950408889634f;
constexpr float QSCALE = 0.25f * LOG2E;

constexpr unsigned WS_EPOCH = 0, WS_FB = 4096, WS_FOB = 8192, WS_FA = 16384;
constexpr unsigned WS_Q = 1u << 20, WS_K = 3u << 20, WS_V = 5u << 20, WS_PQ = 7u << 20, WS_OA = 9u << 20, WS_OB = 11u << 20, WS_KVP = 13u << 20, WS_BYTES = 15u << 20;
constexpr unsigned KVP_STRIDE = 1152;
constexpr unsigned L_EX = 131072, L_RED = L_EX, L_CTL = L_EX + 28672, LDS_BYTES = 163840;
constexpr unsigned C_P3 = 0, C_P5 = 4, C_QRD = 8, C_ALL = 12, C_RDY = 16, C_X = 20, C_FB = 24, C_RED = 28, C_OB = 32, C_WA = 36, C_S3 = 40, C_PB = 48, C_POB = 52, C_SEEN = 64;
constexpr unsigned POISON = 0xffffffffu;
constexpr int CODE_KB = 15;

#define LOADER \
  "s_mov_b64 s[40:41], %[xa]\n" \
  "s_mov_b64 s[42:43], %[wa]\n" \
  "s_mov_b32 s44, 0\n" \
  "s_mov_b32 s45, %[rot]\n" \
  "s_mov_b32 s46, %[d]\n" \
  "s_lshl_b32 s47, s45, 7\n" \
  "s_mul_i32 s48, s45, 0x30000\n" \
  "v_add_u32 v0, s47, %[x0]\n" \
  "v_add_u32 v1, s47, %[x1]\n" \
  "v_add_u32 v2, s48, %[w0]\n" \
  "v_add_u32 v3, s48, %[w1]\n" \
  "s_mov_b32 m0, s46\n" \
  "s_nop 0\n" \
  "global_load_lds_dwordx4 v0, s[40:41] offset:0\n" \
  "s_add_u32 s45, s45, 1\n" \
  "s_and_b32 s45, s45, 7\n" \
  "s_lshl_b32 s47, s45, 7\n" \
  "s_mul_i32 s48, s45, 0x30000\n" \
  "v_add_u32 v0, s47, %[x0]\n" \
  "s_add_u32 m0, s46, 0x2000\n" \
  "s_nop 0\n" \
  "global_load_lds_dwordx4 v2, s[42:43] offset:0\n" \
  "s_add_u32 m0, s46, 0x3c00\n" \
  "s_nop 0\n" \
  "global_load_lds_dwordx4 v2, s[42:43] offset:1024\n" \
  "s_add_u32 m0, s46, 0x5800\n" \
  "s_nop 0\n" \
  "global_load_lds_dwordx4 v2, s[42:43] offset:2048\n" \
  "v_add_u32 v2, s48, %[w0]\n" \
  "1:\n" \
  "s_add_u32 m0, s46, 0x400\n" \
  "s_nop 0\n" \
  "global_load_lds_dwordx4 v1, s[40:41] offset:0\n" \
  "v_add_u32 v1, s47, %[x1]\n" \
  "s_add_u32 m0, s46, 0x2400\n" \
  "s_nop 0\n" \
  "global_load_lds_dwordx4 v3, s[42:43] offset:0\n" \
  "s_add_u32 s44, s44, 1\n" \
  "s_and_b32 s49, s44, 3\n" \
  "s_lshl_b32 s49, s49, 15\n" \
  "s_add_u32 m0, s46, 0x4000\n" \
  "s_nop 0\n" \
  "global_load_lds_dwordx4 v3, s[42:43] offset:1024\n" \
  "s_add_u32 m0, s46, 0x5c00\n" \
  "s_nop 0\n" \
  "global_load_lds_dwordx4 v3, s[42:43] offset:2048\n" \
  "s_add_u32 s46, s49, %[d]\n" \
  "v_add_u32 v3, s48, %[w1]\n" \
  "s_cmp_eq_u32 s44, 8\n" \
  "s_cselect_b64 s[40:41], %[xb], s[40:41]\n" \
  "s_cselect_b64 s[42:43], %[wb], s[42:43]\n" \
  "s_cmp_lt_u32 s44, 2\n" \
  "s_cbranch_scc1 2f\n" \
  "s_waitcnt vmcnt(8)\n" \
  "s_barrier\n" \
  "2:\n" \
  "s_mov_b32 m0, s46\n" \
  "s_nop 0\n" \
  "global_load_lds_dwordx4 v0, s[40:41] offset:0\n" \
  "s_add_u32 s45, s45, 1\n" \
  "s_and_b32 s45, s45, 7\n" \
  "s_lshl_b32 s47, s45, 7\n" \
  "s_mul_i32 s48, s45, 0x30000\n" \
  "v_add_u32 v0, s47, %[x0]\n" \
  "s_add_u32 m0, s46, 0x2000\n" \
  "s_nop 0\n" \
  "global_load_lds_dwordx4 v2, s[42:43] offset:0\n" \
  "s_add_u32 m0, s46, 0x3c00\n" \
  "s_nop 0\n" \
  "global_load_lds_dwordx4 v2, s[42:43] offset:1024\n" \
  "s_add_u32 m0, s46, 0x5800\n" \
  "s_nop 0\n" \
  "global_load_lds_dwordx4 v2, s[42:43] offset:2048\n" \
  "v_add_u32 v2, s48, %[w0]\n" \
  "s_cmp_lg_u32 s44, 15\n" \
  "s_cbranch_scc1 1b\n" \
  "s_add_u32 m0, s46, 0x400\n" \
  "s_nop 0\n" \
  "global_load_lds_dwordx4 v1, s[40:41] offset:0\n" \
  "v_add_u32 v1, s47, %[x1]\n" \
  "s_add_u32 m0, s46, 0x2400\n" \
  "s_nop 0\n" \
  "global_load_lds_dwordx4 v3, s[42:43] offset:0\n" \
  "s_add_u32 s44, s44, 1\n" \
  "s_and_b32 s49, s44, 3\n" \
  "s_lshl_b32 s49, s49, 15\n" \
  "s_add_u32 m0, s46, 0x4000\n" \
  "s_nop 0\n" \
  "global_load_lds_dwordx4 v3, s[42:43] offset:1024\n" \
  "s_add_u32 m0, s46, 0x5c00\n" \
  "s_nop 0\n" \
  "global_load_lds_dwordx4 v3, s[42:43] offset:2048\n"
#define PROJ \
  "s_mov_b32 s40, 0x8000\n" \
  "s_mov_b32 s41, 0xfffe8000\n" \
  "s_mov_b32 s42, 0\n" \
  "v_mov_b64 v[0:1], 0\n" \
  "v_mov_b64 v[2:3], 0\n" \
  "v_mov_b64 v[4:5], 0\n" \
  "v_mov_b64 v[6:7], 0\n" \
  "v_mov_b64 v[8:9], 0\n" \
  "v_mov_b64 v[10:11], 0\n" \
  "v_mov_b64 v[12:13], 0\n" \
  "v_mov_b64 v[14:15], 0\n" \
  "v_mov_b64 v[16:17], 0\n" \
  "v_mov_b64 v[18:19], 0\n" \
  "v_mov_b64 v[20:21], 0\n" \
  "v_mov_b64 v[22:23], 0\n" \
  "v_mov_b64 v[24:25], 0\n" \
  "v_mov_b64 v[26:27], 0\n" \
  "v_mov_b64 v[28:29], 0\n" \
  "v_mov_b64 v[30:31], 0\n" \
  "v_mov_b64 v[32:33], 0\n" \
  "v_mov_b64 v[34:35], 0\n" \
  "v_mov_b64 v[36:37], 0\n" \
  "v_mov_b64 v[38:39], 0\n" \
  "v_mov_b64 v[40:41], 0\n" \
  "v_mov_b64 v[42:43], 0\n" \
  "v_mov_b64 v[44:45], 0\n" \
  "v_mov_b64 v[46:47], 0\n" \
  "s_barrier\n" \
  "ds_read_b128 v[48:51], v112 offset:0\n" \
  "ds_read_b64_tr_b16 v[56:57], v114 offset:0\n" \
  "ds_read_b64_tr_b16 v[58:59], v114 offset:512\n" \
  "ds_read_b128 v[52:55], v112 offset:2048\n" \
  "ds_read_b64_tr_b16 v[60:61], v115 offset:0\n" \
  "ds_read_b64_tr_b16 v[62:63], v115 offset:512\n" \
  "ds_read_b64_tr_b16 v[64:65], v114 offset:8192\n" \
  "ds_read_b64_tr_b16 v[66:67], v114 offset:8704\n" \
  "ds_read_b64_tr_b16 v[68:69], v115 offset:8192\n" \
  "ds_read_b64_tr_b16 v[70:71], v115 offset:8704\n" \
  "ds_read_b64_tr_b16 v[72:73], v114 offset:16384\n" \
  "ds_read_b64_tr_b16 v[74:75], v114 offset:16896\n" \
  "ds_read_b64_tr_b16 v[76:77], v115 offset:16384\n" \
  "ds_read_b64_tr_b16 v[78:79], v115 offset:16896\n" \
  "1:\n" \
  "s_and_b32 s43, s42, 3\n" \
  "s_cmp_eq_u32 s43, 3\n" \
  "s_cselect_b32 s44, s41, s40\n" \
  "s_add_u32 s42, s42, 1\n" \
  "s_waitcnt lgkmcnt(6)\n" \
  "v_mfma_f32_16x16x32_bf16 v[0:3], v[56:59], v[48:51], v[0:3]\n" \
  "ds_read_b128 v[80:83], v113 offset:0\n" \
  "ds_read_b64_tr_b16 v[88:89], v114 offset:4096\n" \
  "v_mfma_f32_16x16x32_bf16 v[4:7], v[56:59], v[52:55], v[4:7]\n" \
  "ds_read_b64_tr_b16 v[90:91], v114 offset:4608\n" \
  "ds_read_b128 v[84:87], v113 offset:2048\n" \
  "v_mfma_f32_16x16x32_bf16 v[8:11], v[60:63], v[48:51], v[8:11]\n" \
  "ds_read_b64_tr_b16 v[92:93], v115 offset:4096\n" \
  "ds_read_b64_tr_b16 v[94:95], v115 offset:4608\n" \
  "v_mfma_f32_16x16x32_bf16 v[12:15], v[60:63], v[52:55], v[12:15]\n" \
  "ds_read_b64_tr_b16 v[96:97], v114 offset:12288\n" \
  "ds_read_b64_tr_b16 v[98:99], v114 offset:12800\n" \
  "s_waitcnt lgkmcnt(8)\n" \
  "v_mfma_f32_16x16x32_bf16 v[16:19], v[64:67], v[48:51], v[16:19]\n" \
  "ds_read_b64_tr_b16 v[100:101], v115 offset:12288\n" \
  "ds_read_b64_tr_b16 v[102:103], v115 offset:12800\n" \
  "v_mfma_f32_16x16x32_bf16 v[20:23], v[64:67], v[52:55], v[20:23]\n" \
  "ds_read_b64_tr_b16 v[104:105], v114 offset:20480\n" \
  "ds_read_b64_tr_b16 v[106:107], v114 offset:20992\n" \
  "v_mfma_f32_16x16x32_bf16 v[24:27], v[68:71], v[48:51], v[24:27]\n" \
  "ds_read_b64_tr_b16 v[108:109], v115 offset:20480\n" \
  "ds_read_b64_tr_b16 v[110:111], v115 offset:20992\n" \
  "v_mfma_f32_16x16x32_bf16 v[28:31], v[68:71], v[52:55], v[28:31]\n" \
  "v_add_u32 v112, s44, v112\n" \
  "v_add_u32 v113, s44, v113\n" \
  "v_add_u32 v114, s44, v114\n" \
  "v_add_u32 v115, s44, v115\n" \
  "v_mfma_f32_16x16x32_bf16 v[32:35], v[48:51], v[72:75], v[32:35]\n" \
  "v_mfma_f32_16x16x32_bf16 v[36:39], v[52:55], v[72:75], v[36:39]\n" \
  "v_mfma_f32_16x16x32_bf16 v[40:43], v[48:51], v[76:79], v[40:43]\n" \
  "v_mfma_f32_16x16x32_bf16 v[44:47], v[52:55], v[76:79], v[44:47]\n" \
  "s_waitcnt lgkmcnt(0)\n" \
  "s_barrier\n" \
  "v_mfma_f32_16x16x32_bf16 v[0:3], v[88:91], v[80:83], v[0:3]\n" \
  "ds_read_b128 v[48:51], v112 offset:0\n" \
  "ds_read_b64_tr_b16 v[56:57], v114 offset:0\n" \
  "v_mfma_f32_16x16x32_bf16 v[4:7], v[88:91], v[84:87], v[4:7]\n" \
  "ds_read_b64_tr_b16 v[58:59], v114 offset:512\n" \
  "ds_read_b128 v[52:55], v112 offset:2048\n" \
  "v_mfma_f32_16x16x32_bf16 v[8:11], v[92:95], v[80:83], v[8:11]\n" \
  "ds_read_b64_tr_b16 v[60:61], v115 offset:0\n" \
  "ds_read_b64_tr_b16 v[62:63], v115 offset:512\n" \
  "v_mfma_f32_16x16x32_bf16 v[12:15], v[92:95], v[84:87], v[12:15]\n" \
  "ds_read_b64_tr_b16 v[64:65], v114 offset:8192\n" \
  "ds_read_b64_tr_b16 v[66:67], v114 offset:8704\n" \
  "v_mfma_f32_16x16x32_bf16 v[16:19], v[96:99], v[80:83], v[16:19]\n" \
  "ds_read_b64_tr_b16 v[68:69], v115 offset:8192\n" \
  "ds_read_b64_tr_b16 v[70:71], v115 offset:8704\n" \
  "v_mfma_f32_16x16x32_bf16 v[20:23], v[96:99], v[84:87], v[20:23]\n" \
  "ds_read_b64_tr_b16 v[72:73], v114 offset:16384\n" \
  "ds_read_b64_tr_b16 v[74:75], v114 offset:16896\n" \
  "v_mfma_f32_16x16x32_bf16 v[24:27], v[100:103], v[80:83], v[24:27]\n" \
  "ds_read_b64_tr_b16 v[76:77], v115 offset:16384\n" \
  "ds_read_b64_tr_b16 v[78:79], v115 offset:16896\n" \
  "v_mfma_f32_16x16x32_bf16 v[28:31], v[100:103], v[84:87], v[28:31]\n" \
  "v_mfma_f32_16x16x32_bf16 v[32:35], v[80:83], v[104:107], v[32:35]\n" \
  "v_mfma_f32_16x16x32_bf16 v[36:39], v[84:87], v[104:107], v[36:39]\n" \
  "v_mfma_f32_16x16x32_bf16 v[40:43], v[80:83], v[108:111], v[40:43]\n" \
  "v_mfma_f32_16x16x32_bf16 v[44:47], v[84:87], v[108:111], v[44:47]\n" \
  "s_cmp_lg_u32 s42, 8\n" \
  "s_cbranch_scc1 1b\n" \
  "v_mov_b32 v120, 0\n" \
  "s_mov_b32 s44, 0x3eb8aa3b\n" \
  "s_mov_b32 s45, s44\n" \
  "global_load_dwordx2 v[120:121], v120, %[ws] sc1\n" \
  "s_add_u32 s46, %[fo], 0x300000\n" \
  "s_add_u32 s47, %[fo], 0x310000\n" \
  "s_add_u32 s48, %[fo], 0x500000\n" \
  "s_add_u32 s51, %[fo], 0x510000\n" \
  "s_add_u32 s49, %[fo], 0x100000\n" \
  "s_add_u32 s50, %[fo], 0x110000\n" \
  "v_cvt_pk_bf16_f32 v132, v16, v17\n" \
  "v_cvt_pk_bf16_f32 v133, v18, v19\n" \
  "v_cvt_pk_bf16_f32 v134, v20, v21\n" \
  "v_cvt_pk_bf16_f32 v135, v22, v23\n" \
  "v_cvt_pk_bf16_f32 v136, v24, v25\n" \
  "v_cvt_pk_bf16_f32 v137, v26, v27\n" \
  "v_cvt_pk_bf16_f32 v138, v28, v29\n" \
  "v_cvt_pk_bf16_f32 v139, v30, v31\n" \
  "s_nop 0\n" \
  "v_permlane16_swap_b32 v132, v134\n" \
  "v_permlane16_swap_b32 v133, v135\n" \
  "v_permlane16_swap_b32 v136, v138\n" \
  "v_permlane16_swap_b32 v137, v139\n" \
  "buffer_store_dwordx4 v[132:135], v117, %[rs], s46 offen sc1\n" \
  "buffer_store_dwordx4 v[136:139], v117, %[rs], s47 offen sc1\n" \
  "v_cvt_pk_bf16_f32 v140, v32, v33\n" \
  "v_cvt_pk_bf16_f32 v141, v34, v35\n" \
  "v_cvt_pk_bf16_f32 v142, v36, v37\n" \
  "v_cvt_pk_bf16_f32 v143, v38, v39\n" \
  "v_cvt_pk_bf16_f32 v144, v40, v41\n" \
  "v_cvt_pk_bf16_f32 v145, v42, v43\n" \
  "v_cvt_pk_bf16_f32 v146, v44, v45\n" \
  "v_cvt_pk_bf16_f32 v147, v46, v47\n" \
  "buffer_store_dwordx4 v[140:143], v118, %[rs], s48 offen sc1\n" \
  "buffer_store_dwordx4 v[144:147], v118, %[rs], s51 offen sc1\n" \
  "v_pk_mul_f32 v[0:1], v[0:1], s[44:45]\n" \
  "v_pk_mul_f32 v[2:3], v[2:3], s[44:45]\n" \
  "v_pk_mul_f32 v[4:5], v[4:5], s[44:45]\n" \
  "v_pk_mul_f32 v[6:7], v[6:7], s[44:45]\n" \
  "v_pk_mul_f32 v[8:9], v[8:9], s[44:45]\n" \
  "v_pk_mul_f32 v[10:11], v[10:11], s[44:45]\n" \
  "v_pk_mul_f32 v[12:13], v[12:13], s[44:45]\n" \
  "v_pk_mul_f32 v[14:15], v[14:15], s[44:45]\n" \
  "v_cvt_pk_bf16_f32 v124, v0, v1\n" \
  "v_cvt_pk_bf16_f32 v125, v2, v3\n" \
  "v_cvt_pk_bf16_f32 v126, v4, v5\n" \
  "v_cvt_pk_bf16_f32 v127, v6, v7\n" \
  "v_cvt_pk_bf16_f32 v128, v8, v9\n" \
  "v_cvt_pk_bf16_f32 v129, v10, v11\n" \
  "v_cvt_pk_bf16_f32 v130, v12, v13\n" \
  "v_cvt_pk_bf16_f32 v131, v14, v15\n" \
  "s_nop 0\n" \
  "v_permlane16_swap_b32 v124, v126\n" \
  "v_permlane16_swap_b32 v125, v127\n" \
  "v_permlane16_swap_b32 v128, v130\n" \
  "v_permlane16_swap_b32 v129, v131\n" \
  "buffer_store_dwordx4 v[124:127], v119, %[rs], s49 offen sc1\n" \
  "buffer_store_dwordx4 v[128:131], v119, %[rs], s50 offen sc1\n" \
  "v_mov_b64 v[0:1], 0\n" \
  "v_mov_b64 v[2:3], 0\n" \
  "v_mov_b64 v[4:5], 0\n" \
  "v_mov_b64 v[6:7], 0\n" \
  "v_mov_b64 v[8:9], 0\n" \
  "v_mov_b64 v[10:11], 0\n" \
  "v_mov_b64 v[12:13], 0\n" \
  "v_mov_b64 v[14:15], 0\n" \
  "v_mov_b64 v[16:17], 0\n" \
  "v_mov_b64 v[18:19], 0\n" \
  "v_mov_b64 v[20:21], 0\n" \
  "v_mov_b64 v[22:23], 0\n" \
  "v_mov_b64 v[24:25], 0\n" \
  "v_mov_b64 v[26:27], 0\n" \
  "v_mov_b64 v[28:29], 0\n" \
  "v_mov_b64 v[30:31], 0\n" \
  "v_mov_b64 v[32:33], 0\n" \
  "v_mov_b64 v[34:35], 0\n" \
  "v_mov_b64 v[36:37], 0\n" \
  "v_mov_b64 v[38:39], 0\n" \
  "v_mov_b64 v[40:41], 0\n" \
  "v_mov_b64 v[42:43], 0\n" \
  "v_mov_b64 v[44:45], 0\n" \
  "v_mov_b64 v[46:47], 0\n" \
  "s_mov_b32 s45, 0\n" \
  "2:\n" \
  "s_and_b32 s43, s42, 3\n" \
  "s_cmp_eq_u32 s43, 3\n" \
  "s_cselect_b32 s44, s41, s40\n" \
  "s_add_u32 s42, s42, 1\n" \
  "s_waitcnt lgkmcnt(6)\n" \
  "v_mfma_f32_16x16x32_bf16 v[0:3], v[56:59], v[48:51], v[0:3]\n" \
  "ds_read_b128 v[80:83], v113 offset:0\n" \
  "ds_read_b64_tr_b16 v[88:89], v114 offset:4096\n" \
  "v_mfma_f32_16x16x32_bf16 v[4:7], v[56:59], v[52:55], v[4:7]\n" \
  "ds_read_b64_tr_b16 v[90:91], v114 offset:4608\n" \
  "ds_read_b128 v[84:87], v113 offset:2048\n" \
  "v_mfma_f32_16x16x32_bf16 v[8:11], v[60:63], v[48:51], v[8:11]\n" \
  "ds_read_b64_tr_b16 v[92:93], v115 offset:4096\n" \
  "ds_read_b64_tr_b16 v[94:95], v115 offset:4608\n" \
  "v_mfma_f32_16x16x32_bf16 v[12:15], v[60:63], v[52:55], v[12:15]\n" \
  "ds_read_b64_tr_b16 v[96:97], v114 offset:12288\n" \
  "ds_read_b64_tr_b16 v[98:99], v114 offset:12800\n" \
  "s_waitcnt lgkmcnt(8)\n" \
  "v_mfma_f32_16x16x32_bf16 v[16:19], v[48:51], v[64:67], v[16:19]\n" \
  "ds_read_b64_tr_b16 v[100:101], v115 offset:12288\n" \
  "ds_read_b64_tr_b16 v[102:103], v115 offset:12800\n" \
  "v_mfma_f32_16x16x32_bf16 v[20:23], v[52:55], v[64:67], v[20:23]\n" \
  "ds_read_b64_tr_b16 v[104:105], v114 offset:20480\n" \
  "ds_read_b64_tr_b16 v[106:107], v114 offset:20992\n" \
  "v_mfma_f32_16x16x32_bf16 v[24:27], v[48:51], v[68:71], v[24:27]\n" \
  "ds_read_b64_tr_b16 v[108:109], v115 offset:20480\n" \
  "ds_read_b64_tr_b16 v[110:111], v115 offset:20992\n" \
  "v_mfma_f32_16x16x32_bf16 v[28:31], v[52:55], v[68:71], v[28:31]\n" \
  "v_add_u32 v112, s44, v112\n" \
  "v_add_u32 v113, s44, v113\n" \
  "v_add_u32 v114, s44, v114\n" \
  "v_add_u32 v115, s44, v115\n" \
  "v_mfma_f32_16x16x32_bf16 v[32:35], v[48:51], v[72:75], v[32:35]\n" \
  "v_mfma_f32_16x16x32_bf16 v[36:39], v[52:55], v[72:75], v[36:39]\n" \
  "v_mfma_f32_16x16x32_bf16 v[40:43], v[48:51], v[76:79], v[40:43]\n" \
  "v_mfma_f32_16x16x32_bf16 v[44:47], v[52:55], v[76:79], v[44:47]\n" \
  "s_getreg_b32 s43, hwreg(HW_REG_IB_STS, 0, 4)\n" \
  "s_or_b32 s43, s43, s45\n" \
  "s_cmp_lg_u32 s43, 0\n" \
  "s_cbranch_scc0 5f\n" \
  "4:\n" \
  "s_waitcnt lgkmcnt(0)\n" \
  "s_barrier\n" \
  "s_cmp_eq_u32 s42, 16\n" \
  "s_cbranch_scc1 3f\n" \
  "v_mfma_f32_16x16x32_bf16 v[0:3], v[88:91], v[80:83], v[0:3]\n" \
  "ds_read_b128 v[48:51], v112 offset:0\n" \
  "ds_read_b64_tr_b16 v[56:57], v114 offset:0\n" \
  "v_mfma_f32_16x16x32_bf16 v[4:7], v[88:91], v[84:87], v[4:7]\n" \
  "ds_read_b64_tr_b16 v[58:59], v114 offset:512\n" \
  "ds_read_b128 v[52:55], v112 offset:2048\n" \
  "v_mfma_f32_16x16x32_bf16 v[8:11], v[92:95], v[80:83], v[8:11]\n" \
  "ds_read_b64_tr_b16 v[60:61], v115 offset:0\n" \
  "ds_read_b64_tr_b16 v[62:63], v115 offset:512\n" \
  "v_mfma_f32_16x16x32_bf16 v[12:15], v[92:95], v[84:87], v[12:15]\n" \
  "ds_read_b64_tr_b16 v[64:65], v114 offset:8192\n" \
  "ds_read_b64_tr_b16 v[66:67], v114 offset:8704\n" \
  "v_mfma_f32_16x16x32_bf16 v[16:19], v[80:83], v[96:99], v[16:19]\n" \
  "ds_read_b64_tr_b16 v[68:69], v115 offset:8192\n" \
  "ds_read_b64_tr_b16 v[70:71], v115 offset:8704\n" \
  "v_mfma_f32_16x16x32_bf16 v[20:23], v[84:87], v[96:99], v[20:23]\n" \
  "ds_read_b64_tr_b16 v[72:73], v114 offset:16384\n" \
  "ds_read_b64_tr_b16 v[74:75], v114 offset:16896\n" \
  "v_mfma_f32_16x16x32_bf16 v[24:27], v[80:83], v[100:103], v[24:27]\n" \
  "ds_read_b64_tr_b16 v[76:77], v115 offset:16384\n" \
  "ds_read_b64_tr_b16 v[78:79], v115 offset:16896\n" \
  "v_mfma_f32_16x16x32_bf16 v[28:31], v[84:87], v[100:103], v[28:31]\n" \
  "v_mfma_f32_16x16x32_bf16 v[32:35], v[80:83], v[104:107], v[32:35]\n" \
  "v_mfma_f32_16x16x32_bf16 v[36:39], v[84:87], v[104:107], v[36:39]\n" \
  "v_mfma_f32_16x16x32_bf16 v[40:43], v[80:83], v[108:111], v[40:43]\n" \
  "v_mfma_f32_16x16x32_bf16 v[44:47], v[84:87], v[108:111], v[44:47]\n" \
  "s_branch 2b\n" \
  "5:\n" \
  "s_waitcnt vmcnt(0)\n" \
  "v_lshl_add_u64 v[120:121], v[120:121], 0, 1\n" \
  "s_mov_b64 exec, 1\n" \
  "global_store_dwordx2 v122, v[120:121], %[ws] sc1\n" \
  "s_mov_b64 exec, -1\n" \
  "s_mov_b32 s45, 1\n" \
  "s_branch 4b\n" \
  "3:\n" \
  "v_mfma_f32_16x16x32_bf16 v[0:3], v[88:91], v[80:83], v[0:3]\n" \
  "v_mfma_f32_16x16x32_bf16 v[4:7], v[88:91], v[84:87], v[4:7]\n" \
  "v_mfma_f32_16x16x32_bf16 v[8:11], v[92:95], v[80:83], v[8:11]\n" \
  "v_mfma_f32_16x16x32_bf16 v[12:15], v[92:95], v[84:87], v[12:15]\n" \
  "v_mfma_f32_16x16x32_bf16 v[16:19], v[80:83], v[96:99], v[16:19]\n" \
  "v_mfma_f32_16x16x32_bf16 v[20:23], v[84:87], v[96:99], v[20:23]\n" \
  "v_mfma_f32_16x16x32_bf16 v[24:27], v[80:83], v[100:103], v[24:27]\n" \
  "v_mfma_f32_16x16x32_bf16 v[28:31], v[84:87], v[100:103], v[28:31]\n" \
  "v_mfma_f32_16x16x32_bf16 v[32:35], v[80:83], v[104:107], v[32:35]\n" \
  "v_mfma_f32_16x16x32_bf16 v[36:39], v[84:87], v[104:107], v[36:39]\n" \
  "v_mfma_f32_16x16x32_bf16 v[40:43], v[80:83], v[108:111], v[40:43]\n" \
  "v_mfma_f32_16x16x32_bf16 v[44:47], v[84:87], v[108:111], v[44:47]\n" \
  "s_cmp_lg_u32 s45, 0\n" \
  "s_cbranch_scc1 6f\n" \
  "s_waitcnt vmcnt(0)\n" \
  "v_lshl_add_u64 v[120:121], v[120:121], 0, 1\n" \
  "s_mov_b64 exec, 1\n" \
  "global_store_dwordx2 v122, v[120:121], %[ws] sc1\n" \
  "s_mov_b64 exec, -1\n" \
  "s_mov_b32 s45, 1\n" \
  "6:\n" \
  "s_nop 7\n"
#define ATTN_INIT \
  "v_mov_b32 v112, v131\n" \
  "v_mov_b32 v113, v131\n" \
  "v_mov_b32 v114, v131\n" \
  "v_mov_b32 v115, v131\n" \
  "v_mov_b32 v116, v131\n" \
  "v_mov_b32 v117, v131\n" \
  "v_mov_b32 v118, v131\n" \
  "v_mov_b32 v119, v131\n" \
  "v_mov_b32 v120, v131\n" \
  "v_mov_b32 v121, v131\n" \
  "v_mov_b32 v122, v131\n" \
  "v_mov_b32 v123, v131\n" \
  "v_mov_b32 v124, v131\n" \
  "v_mov_b32 v125, v131\n" \
  "v_mov_b32 v126, v131\n" \
  "v_mov_b32 v127, v131\n" \
  "v_mov_b64 v[24:25], 0\n" \
  "v_mov_b64 v[26:27], 0\n" \
  "v_mov_b64 v[28:29], 0\n" \
  "v_mov_b64 v[30:31], 0\n" \
  "v_mov_b64 v[32:33], 0\n" \
  "v_mov_b64 v[34:35], 0\n" \
  "v_mov_b64 v[36:37], 0\n" \
  "v_mov_b64 v[38:39], 0\n" \
  "v_mov_b64 v[40:41], 0\n" \
  "v_mov_b64 v[42:43], 0\n" \
  "v_mov_b64 v[44:45], 0\n" \
  "v_mov_b64 v[46:47], 0\n" \
  "v_mov_b64 v[48:49], 0\n" \
  "v_mov_b64 v[50:51], 0\n" \
  "v_mov_b64 v[52:53], 0\n" \
  "v_mov_b64 v[54:55], 0\n" \
  "v_mov_b64 v[56:57], 0\n" \
  "v_mov_b64 v[58:59], 0\n" \
  "v_mov_b64 v[60:61], 0\n" \
  "v_mov_b64 v[62:63], 0\n" \
  "v_mov_b32 v128, 0xdddddddd\n" \
  "v_mov_b32 v132, 1\n"
#define ATTN_LOOP \
  "s_mov_b32 s40, 0xffff\n" \
  "s_mov_b32 s41, 0xffff\n" \
  "ds_read_b128 v[96:99], v137\n" \
  "ds_read_b128 v[16:19], v133\n" \
  "ds_read_b128 v[104:107], v129\n" \
  "ds_read_b128 v[100:103], v137 offset:1024\n" \
  "ds_read_b128 v[108:111], v129 offset:1024\n" \
  "s_mov_b64 exec, s[40:41]\n" \
  "ds_read_b128 v[112:115], v130\n" \
  "ds_read_b128 v[116:119], v130 offset:512\n" \
  "s_mov_b64 exec, -1\n" \
  "s_mov_b32 s42, 0\n" \
  "s_mov_b32 s43, 0x80008000\n" \
  "s_mov_b32 s44, 120\n" \
  "s_setprio 2\n" \
  "s_waitcnt lgkmcnt(5)\n" \
  "v_xor_b32 v16, s43, v16\n" \
  "v_xor_b32 v17, s43, v17\n" \
  "v_xor_b32 v18, s43, v18\n" \
  "v_xor_b32 v19, s43, v19\n" \
  "s_waitcnt lgkmcnt(3)\n" \
  "s_nop 0\n" \
  "v_mfma_f32_32x32x16_bf16 v[64:79], v[16:19], v[96:99], 0\n" \
  "v_mfma_f32_32x32x16_bf16 v[0:15], v[104:107], v[96:99], v[64:79]\n" \
  "v_mfma_f32_32x32x16_bf16 v[80:95], v[16:19], v[100:103], 0\n" \
  "s_mov_b64 exec, 1\n" \
  "ds_add_u32 v134, v132 offset:8\n" \
  "s_mov_b64 exec, -1\n" \
  "s_nop 7\n" \
  "1:\n" \
  "v_exp_f32 v0, v0\n" \
  "v_exp_f32 v1, v1\n" \
  "v_smfmac_f32_32x32x32_bf16 v[48:63], v[124:127], v[24:31], v128\n" \
  "s_waitcnt lgkmcnt(0)\n" \
  "s_mov_b64 exec, s[40:41]\n" \
  "ds_read_b128 v[120:123], v130 offset:1024\n" \
  "ds_read_b128 v[124:127], v130 offset:1536\n" \
  "s_mov_b64 exec, -1\n" \
  "v_exp_f32 v2, v2\n" \
  "v_exp_f32 v3, v3\n" \
  "v_exp_f32 v4, v4\n" \
  "v_mfma_f32_32x32x16_bf16 v[16:31], v[104:107], v[100:103], v[80:95]\n" \
  "ds_read_b128 v[104:107], v129 offset:2048\n" \
  "v_exp_f32 v5, v5\n" \
  "v_exp_f32 v6, v6\n" \
  "v_exp_f32 v7, v7\n" \
  "v_exp_f32 v8, v8\n" \
  "v_exp_f32 v9, v9\n" \
  "v_smfmac_f32_32x32x32_bf16 v[32:47], v[112:115], v[0:7], v128\n" \
  "v_exp_f32 v10, v10\n" \
  "v_exp_f32 v11, v11\n" \
  "v_exp_f32 v12, v12\n" \
  "v_exp_f32 v13, v13\n" \
  "v_exp_f32 v14, v14\n" \
  "v_exp_f32 v15, v15\n" \
  "v_exp_f32 v16, v16\n" \
  "v_exp_f32 v17, v17\n" \
  "v_smfmac_f32_32x32x32_bf16 v[32:47], v[116:119], v[8:15], v128\n" \
  "v_exp_f32 v18, v18\n" \
  "v_exp_f32 v19, v19\n" \
  "v_exp_f32 v20, v20\n" \
  "v_mfma_f32_32x32x16_bf16 v[0:15], v[108:111], v[96:99], v[64:79]\n" \
  "v_exp_f32 v21, v21\n" \
  "v_exp_f32 v22, v22\n" \
  "v_exp_f32 v23, v23\n" \
  "v_exp_f32 v24, v24\n" \
  "v_exp_f32 v25, v25\n" \
  "v_smfmac_f32_32x32x32_bf16 v[48:63], v[112:115], v[16:23], v128\n" \
  "v_exp_f32 v26, v26\n" \
  "v_exp_f32 v27, v27\n" \
  "v_exp_f32 v28, v28\n" \
  "v_exp_f32 v29, v29\n" \
  "v_exp_f32 v30, v30\n" \
  "v_exp_f32 v31, v31\n" \
  "v_exp_f32 v0, v0\n" \
  "v_exp_f32 v1, v1\n" \
  "v_smfmac_f32_32x32x32_bf16 v[48:63], v[116:119], v[24:31], v128\n" \
  "s_waitcnt lgkmcnt(0)\n" \
  "s_mov_b64 exec, s[40:41]\n" \
  "ds_read_b128 v[112:115], v130 offset:2048\n" \
  "ds_read_b128 v[116:119], v130 offset:2560\n" \
  "s_mov_b64 exec, -1\n" \
  "v_exp_f32 v2, v2\n" \
  "v_exp_f32 v3, v3\n" \
  "v_exp_f32 v4, v4\n" \
  "v_mfma_f32_32x32x16_bf16 v[16:31], v[108:111], v[100:103], v[80:95]\n" \
  "ds_read_b128 v[108:111], v129 offset:3072\n" \
  "v_exp_f32 v5, v5\n" \
  "v_exp_f32 v6, v6\n" \
  "v_exp_f32 v7, v7\n" \
  "v_exp_f32 v8, v8\n" \
  "v_exp_f32 v9, v9\n" \
  "v_smfmac_f32_32x32x32_bf16 v[32:47], v[120:123], v[0:7], v128\n" \
  "v_exp_f32 v10, v10\n" \
  "v_exp_f32 v11, v11\n" \
  "v_exp_f32 v12, v12\n" \
  "v_exp_f32 v13, v13\n" \
  "v_exp_f32 v14, v14\n" \
  "v_exp_f32 v15, v15\n" \
  "s_cmp_eq_u32 s42, 1\n" \
  "s_cbranch_scc1 8f\n" \
  "9:\n" \
  "v_exp_f32 v16, v16\n" \
  "v_exp_f32 v17, v17\n" \
  "v_smfmac_f32_32x32x32_bf16 v[32:47], v[124:127], v[8:15], v128\n" \
  "v_exp_f32 v18, v18\n" \
  "v_exp_f32 v19, v19\n" \
  "v_exp_f32 v20, v20\n" \
  "v_mfma_f32_32x32x16_bf16 v[0:15], v[104:107], v[96:99], v[64:79]\n" \
  "v_exp_f32 v21, v21\n" \
  "v_exp_f32 v22, v22\n" \
  "v_exp_f32 v23, v23\n" \
  "v_exp_f32 v24, v24\n" \
  "v_exp_f32 v25, v25\n" \
  "v_smfmac_f32_32x32x32_bf16 v[48:63], v[120:123], v[16:23], v128\n" \
  "v_exp_f32 v26, v26\n" \
  "v_exp_f32 v27, v27\n" \
  "v_exp_f32 v28, v28\n" \
  "v_exp_f32 v29, v29\n" \
  "v_exp_f32 v30, v30\n" \
  "v_exp_f32 v31, v31\n" \
  "v_exp_f32 v0, v0\n" \
  "v_exp_f32 v1, v1\n" \
  "v_smfmac_f32_32x32x32_bf16 v[48:63], v[124:127], v[24:31], v128\n" \
  "s_waitcnt lgkmcnt(0)\n" \
  "s_mov_b64 exec, s[40:41]\n" \
  "ds_read_b128 v[120:123], v130 offset:3072\n" \
  "ds_read_b128 v[124:127], v130 offset:3584\n" \
  "s_mov_b64 exec, -1\n" \
  "v_exp_f32 v2, v2\n" \
  "v_exp_f32 v3, v3\n" \
  "v_exp_f32 v4, v4\n" \
  "v_mfma_f32_32x32x16_bf16 v[16:31], v[104:107], v[100:103], v[80:95]\n" \
  "ds_read_b128 v[104:107], v129 offset:16384\n" \
  "v_exp_f32 v5, v5\n" \
  "v_exp_f32 v6, v6\n" \
  "v_exp_f32 v7, v7\n" \
  "v_exp_f32 v8, v8\n" \
  "v_exp_f32 v9, v9\n" \
  "v_smfmac_f32_32x32x32_bf16 v[32:47], v[112:115], v[0:7], v128\n" \
  "v_exp_f32 v10, v10\n" \
  "v_exp_f32 v11, v11\n" \
  "v_exp_f32 v12, v12\n" \
  "v_exp_f32 v13, v13\n" \
  "v_exp_f32 v14, v14\n" \
  "v_exp_f32 v15, v15\n" \
  "v_exp_f32 v16, v16\n" \
  "v_exp_f32 v17, v17\n" \
  "v_smfmac_f32_32x32x32_bf16 v[32:47], v[116:119], v[8:15], v128\n" \
  "v_exp_f32 v18, v18\n" \
  "v_exp_f32 v19, v19\n" \
  "v_exp_f32 v20, v20\n" \
  "v_mfma_f32_32x32x16_bf16 v[0:15], v[108:111], v[96:99], v[64:79]\n" \
  "v_exp_f32 v21, v21\n" \
  "v_exp_f32 v22, v22\n" \
  "v_exp_f32 v23, v23\n" \
  "v_exp_f32 v24, v24\n" \
  "v_exp_f32 v25, v25\n" \
  "v_smfmac_f32_32x32x32_bf16 v[48:63], v[112:115], v[16:23], v128\n" \
  "v_exp_f32 v26, v26\n" \
  "v_exp_f32 v27, v27\n" \
  "v_exp_f32 v28, v28\n" \
  "v_exp_f32 v29, v29\n" \
  "v_exp_f32 v30, v30\n" \
  "v_exp_f32 v31, v31\n" \
  "v_exp_f32 v0, v0\n" \
  "v_exp_f32 v1, v1\n" \
  "v_smfmac_f32_32x32x32_bf16 v[48:63], v[116:119], v[24:31], v128\n" \
  "s_waitcnt lgkmcnt(0)\n" \
  "s_bitcmp1_b32 s44, s42\n" \
  "s_cbranch_scc1 6f\n" \
  "5:\n" \
  "s_mov_b64 exec, s[40:41]\n" \
  "ds_read_b128 v[112:115], v130 offset:16384\n" \
  "ds_read_b128 v[116:119], v130 offset:16896\n" \
  "s_mov_b64 exec, -1\n" \
  "v_exp_f32 v2, v2\n" \
  "v_exp_f32 v3, v3\n" \
  "v_exp_f32 v4, v4\n" \
  "v_mfma_f32_32x32x16_bf16 v[16:31], v[108:111], v[100:103], v[80:95]\n" \
  "ds_read_b128 v[108:111], v129 offset:17408\n" \
  "v_exp_f32 v5, v5\n" \
  "v_exp_f32 v6, v6\n" \
  "v_exp_f32 v7, v7\n" \
  "v_exp_f32 v8, v8\n" \
  "v_exp_f32 v9, v9\n" \
  "v_smfmac_f32_32x32x32_bf16 v[32:47], v[120:123], v[0:7], v128\n" \
  "v_exp_f32 v10, v10\n" \
  "v_exp_f32 v11, v11\n" \
  "v_exp_f32 v12, v12\n" \
  "v_exp_f32 v13, v13\n" \
  "v_exp_f32 v14, v14\n" \
  "v_exp_f32 v15, v15\n" \
  "v_exp_f32 v16, v16\n" \
  "v_exp_f32 v17, v17\n" \
  "v_smfmac_f32_32x32x32_bf16 v[32:47], v[124:127], v[8:15], v128\n" \
  "v_exp_f32 v18, v18\n" \
  "v_exp_f32 v19, v19\n" \
  "v_exp_f32 v20, v20\n" \
  "v_mfma_f32_32x32x16_bf16 v[0:15], v[104:107], v[96:99], v[64:79]\n" \
  "v_exp_f32 v21, v21\n" \
  "v_exp_f32 v22, v22\n" \
  "v_exp_f32 v23, v23\n" \
  "v_exp_f32 v24, v24\n" \
  "v_exp_f32 v25, v25\n" \
  "v_smfmac_f32_32x32x32_bf16 v[48:63], v[120:123], v[16:23], v128\n" \
  "v_add_u32 v129, 16384, v129\n" \
  "v_add_u32 v130, 16384, v130\n" \
  "s_add_u32 s42, s42, 1\n" \
  "s_cmp_lg_u32 s42, 8\n" \
  "v_exp_f32 v26, v26\n" \
  "v_exp_f32 v27, v27\n" \
  "v_exp_f32 v28, v28\n" \
  "v_exp_f32 v29, v29\n" \
  "v_exp_f32 v30, v30\n" \
  "v_exp_f32 v31, v31\n" \
  "s_cbranch_scc1 1b\n" \
  "s_cmp_lg_u32 %[kh], 0\n" \
  "s_mov_b32 s43, 0x7149f2ca\n" \
  "v_smfmac_f32_32x32x32_bf16 v[48:63], v[124:127], v[24:31], v128\n" \
  "s_setprio 3\n" \
  "s_cbranch_scc1 4f\n" \
  "s_nop 9\n" \
  "ds_write_b128 v135, v[48:51] offset:0\n" \
  "ds_write_b128 v135, v[52:55] offset:1024\n" \
  "ds_write_b32 v136, v56 offset:0\n" \
  "s_waitcnt lgkmcnt(0)\n" \
  "s_barrier\n" \
  "ds_read_b32 v8, v136 offset:9216\n" \
  "ds_read_b128 v[0:3], v135 offset:9216\n" \
  "ds_read_b128 v[4:7], v135 offset:10240\n" \
  "s_nop 0\n" \
  "s_waitcnt lgkmcnt(2)\n" \
  "v_add_f32 v8, v8, v40\n" \
  "s_waitcnt lgkmcnt(1)\n" \
  "v_rcp_f32 v10, v8\n" \
  "v_pk_add_f32 v[0:1], v[0:1], v[32:33]\n" \
  "v_pk_add_f32 v[2:3], v[2:3], v[34:35]\n" \
  "s_waitcnt lgkmcnt(0)\n" \
  "v_pk_add_f32 v[4:5], v[4:5], v[36:37]\n" \
  "v_pk_add_f32 v[6:7], v[6:7], v[38:39]\n" \
  "v_pk_mul_f32 v[16:17], v[0:1], 0 op_sel_hi:[1,0]\n" \
  "v_pk_mul_f32 v[0:1], v[0:1], v[10:11] op_sel_hi:[1,0]\n" \
  "v_pk_fma_f32 v[16:17], v[2:3], 0, v[16:17] op_sel_hi:[1,0,1]\n" \
  "v_pk_mul_f32 v[2:3], v[2:3], v[10:11] op_sel_hi:[1,0]\n" \
  "v_pk_fma_f32 v[16:17], v[4:5], 0, v[16:17] op_sel_hi:[1,0,1]\n" \
  "v_pk_mul_f32 v[4:5], v[4:5], v[10:11] op_sel_hi:[1,0]\n" \
  "v_pk_fma_f32 v[16:17], v[6:7], 0, v[16:17] op_sel_hi:[1,0,1]\n" \
  "v_pk_mul_f32 v[6:7], v[6:7], v[10:11] op_sel_hi:[1,0]\n" \
  "v_add_f32 v8, v8, v16\n" \
  "v_cvt_pk_bf16_f32 v12, v0, v1\n" \
  "v_add_f32 v8, v8, v17\n" \
  "v_cvt_pk_bf16_f32 v13, v2, v3\n" \
  "v_cmp_gt_f32 vcc, s43, v8\n" \
  "v_cvt_pk_bf16_f32 v14, v4, v5\n" \
  "v_cvt_pk_bf16_f32 v15, v6, v7\n" \
  "s_branch 7f\n" \
  "6:\n" \
  "s_mov_b64 exec, 1\n" \
  "s_cmp_lg_u32 s42, 3\n" \
  "s_cbranch_scc1 61f\n" \
  "ds_add_u32 v134, v132\n" \
  "61:\n" \
  "s_cmp_lg_u32 s42, 5\n" \
  "s_cbranch_scc1 62f\n" \
  "ds_add_u32 v134, v132 offset:4\n" \
  "62:\n" \
  "s_cmp_lg_u32 s42, 4\n" \
  "s_cbranch_scc1 63f\n" \
  "s_setprio 1\n" \
  "63:\n" \
  "s_cmp_lg_u32 s42, 6\n" \
  "s_cbranch_scc1 5b\n" \
  "s_setprio 0\n" \
  "s_branch 5b\n" \
  "8:\n" \
  "ds_read_b32 v133, v134 offset:12\n" \
  "s_waitcnt lgkmcnt(0)\n" \
  "v_readfirstlane_b32 s43, v133\n" \
  "s_nop 3\n" \
  "s_cmp_ge_u32 s43, 4\n" \
  "s_cbranch_scc1 9b\n" \
  "s_sleep 1\n" \
  "s_branch 8b\n" \
  "4:\n" \
  "ds_write_b128 v135, v[32:35] offset:9216\n" \
  "ds_write_b128 v135, v[36:39] offset:10240\n" \
  "ds_write_b32 v136, v40 offset:9216\n" \
  "s_waitcnt lgkmcnt(0)\n" \
  "s_barrier\n" \
  "ds_read_b32 v8, v136 offset:0\n" \
  "ds_read_b128 v[0:3], v135 offset:0\n" \
  "ds_read_b128 v[4:7], v135 offset:1024\n" \
  "s_nop 0\n" \
  "s_waitcnt lgkmcnt(2)\n" \
  "v_add_f32 v8, v8, v56\n" \
  "s_waitcnt lgkmcnt(1)\n" \
  "v_rcp_f32 v10, v8\n" \
  "v_pk_add_f32 v[0:1], v[0:1], v[48:49]\n" \
  "v_pk_add_f32 v[2:3], v[2:3], v[50:51]\n" \
  "s_waitcnt lgkmcnt(0)\n" \
  "v_pk_add_f32 v[4:5], v[4:5], v[52:53]\n" \
  "v_pk_add_f32 v[6:7], v[6:7], v[54:55]\n" \
  "v_pk_mul_f32 v[16:17], v[0:1], 0 op_sel_hi:[1,0]\n" \
  "v_pk_mul_f32 v[0:1], v[0:1], v[10:11] op_sel_hi:[1,0]\n" \
  "v_pk_fma_f32 v[16:17], v[2:3], 0, v[16:17] op_sel_hi:[1,0,1]\n" \
  "v_pk_mul_f32 v[2:3], v[2:3], v[10:11] op_sel_hi:[1,0]\n" \
  "v_pk_fma_f32 v[16:17], v[4:5], 0, v[16:17] op_sel_hi:[1,0,1]\n" \
  "v_pk_mul_f32 v[4:5], v[4:5], v[10:11] op_sel_hi:[1,0]\n" \
  "v_pk_fma_f32 v[16:17], v[6:7], 0, v[16:17] op_sel_hi:[1,0,1]\n" \
  "v_pk_mul_f32 v[6:7], v[6:7], v[10:11] op_sel_hi:[1,0]\n" \
  "v_add_f32 v8, v8, v16\n" \
  "v_cvt_pk_bf16_f32 v12, v0, v1\n" \
  "v_add_f32 v8, v8, v17\n" \
  "v_cvt_pk_bf16_f32 v13, v2, v3\n" \
  "v_cmp_gt_f32 vcc, s43, v8\n" \
  "v_cvt_pk_bf16_f32 v14, v4, v5\n" \
  "v_cvt_pk_bf16_f32 v15, v6, v7\n" \
  "7:\n" \
  "s_cmp_eq_u64 vcc, exec\n" \
  "s_cselect_b32 %[bad], 0, 1\n" \
  "s_cbranch_scc0 3f\n" \
  "buffer_store_dwordx4 v[12:15], v138, %[rs], %[oo] offen sc1\n" \
  "s_nop 1\n" \
  "3:\n"
#define OUT_A \
  "s_add_u32 s40, %[ob], 0x1000\n" \
  "s_mov_b64 s[46:47], -1\n" \
  "s_mov_b32 s42, 200000\n" \
  "ds_read_b64_tr_b16 v[32:33], v130 offset:0\n" \
  "ds_read_b64_tr_b16 v[34:35], v130 offset:1024\n" \
  "ds_read_b64_tr_b16 v[36:37], v131 offset:0\n" \
  "ds_read_b64_tr_b16 v[38:39], v131 offset:1024\n" \
  "ds_read_b64_tr_b16 v[40:41], v130 offset:2048\n" \
  "ds_read_b64_tr_b16 v[42:43], v130 offset:3072\n" \
  "ds_read_b64_tr_b16 v[44:45], v131 offset:2048\n" \
  "ds_read_b64_tr_b16 v[46:47], v131 offset:3072\n" \
  "ds_read_b64_tr_b16 v[48:49], v130 offset:4096\n" \
  "ds_read_b64_tr_b16 v[50:51], v130 offset:5120\n" \
  "ds_read_b64_tr_b16 v[52:53], v131 offset:4096\n" \
  "ds_read_b64_tr_b16 v[54:55], v131 offset:5120\n" \
  "ds_read_b64_tr_b16 v[56:57], v130 offset:6144\n" \
  "ds_read_b64_tr_b16 v[58:59], v130 offset:7168\n" \
  "ds_read_b64_tr_b16 v[60:61], v131 offset:6144\n" \
  "ds_read_b64_tr_b16 v[62:63], v131 offset:7168\n" \
  "ds_read_b64_tr_b16 v[64:65], v130 offset:8192\n" \
  "ds_read_b64_tr_b16 v[66:67], v130 offset:9216\n" \
  "ds_read_b64_tr_b16 v[68:69], v131 offset:8192\n" \
  "ds_read_b64_tr_b16 v[70:71], v131 offset:9216\n" \
  "ds_read_b64_tr_b16 v[72:73], v130 offset:10240\n" \
  "ds_read_b64_tr_b16 v[74:75], v130 offset:11264\n" \
  "ds_read_b64_tr_b16 v[76:77], v131 offset:10240\n" \
  "ds_read_b64_tr_b16 v[78:79], v131 offset:11264\n" \
  "ds_read_b64_tr_b16 v[80:81], v130 offset:12288\n" \
  "ds_read_b64_tr_b16 v[82:83], v130 offset:13312\n" \
  "ds_read_b64_tr_b16 v[84:85], v131 offset:12288\n" \
  "ds_read_b64_tr_b16 v[86:87], v131 offset:13312\n" \
  "ds_read_b64_tr_b16 v[88:89], v130 offset:14336\n" \
  "ds_read_b64_tr_b16 v[90:91], v130 offset:15360\n" \
  "ds_read_b64_tr_b16 v[92:93], v131 offset:14336\n" \
  "ds_read_b64_tr_b16 v[94:95], v131 offset:15360\n" \
  "v_mov_b32 v133, 1\n" \
  "s_mov_b64 exec, 1\n" \
  "s_waitcnt lgkmcnt(0)\n" \
  "ds_add_u32 v132, v133\n" \
  "s_mov_b64 exec, -1\n" \
  "s_waitcnt vmcnt(0) expcnt(0)\n" \
  "buffer_load_dwordx4 v[0:3], v129, %[rs], %[ob] offen offset:0 sc1\n" \
  "buffer_load_dwordx4 v[4:7], v129, %[rs], %[ob] offen offset:1024 sc1\n" \
  "buffer_load_dwordx4 v[8:11], v129, %[rs], %[ob] offen offset:2048 sc1\n" \
  "buffer_load_dwordx4 v[12:15], v129, %[rs], %[ob] offen offset:3072 sc1\n" \
  "buffer_load_dwordx4 v[16:19], v129, %[rs], s40 offen offset:0 sc1\n" \
  "buffer_load_dwordx4 v[20:23], v129, %[rs], s40 offen offset:1024 sc1\n" \
  "buffer_load_dwordx4 v[24:27], v129, %[rs], s40 offen offset:2048 sc1\n" \
  "buffer_load_dwordx4 v[28:31], v129, %[rs], s40 offen offset:3072 sc1\n" \
  "v_add_u32 v137, v129, v134\n" \
  "v_xor_b32 v138, 32, v137\n" \
  "v_xor_b32 v139, 64, v137\n" \
  "v_xor_b32 v140, 96, v137\n" \
  "v_xor_b32 v141, 128, v137\n" \
  "v_xor_b32 v142, 160, v137\n" \
  "v_xor_b32 v143, 192, v137\n" \
  "v_xor_b32 v144, 224, v137\n" \
  "7:\n" \
  "ds_read_b32 v128, v132\n" \
  "s_waitcnt lgkmcnt(0)\n" \
  "v_readfirstlane_b32 s43, v128\n" \
  "s_nop 1\n" \
  "s_cmp_ge_u32 s43, 8\n" \
  "s_cbranch_scc0 7b\n" \
  "s_waitcnt vmcnt(0)\n" \
  "s_getpc_b64 s[44:45]\n" \
  "v_max3_u32 v128, v0, v1, v2\n" \
  "v_max_u32 v128, v128, v3\n" \
  "v_cmp_eq_u32 vcc, -1, v128\n" \
  "s_and_b64 vcc, vcc, s[46:47]\n" \
  "s_cbranch_vccnz 1f\n" \
  "v_mfma_f32_32x32x16_bf16 v[96:111], v[32:35], v[0:3], 0\n" \
  "v_mfma_f32_32x32x16_bf16 v[112:127], v[36:39], v[0:3], 0\n" \
  "s_getpc_b64 s[44:45]\n" \
  "v_max3_u32 v128, v4, v5, v6\n" \
  "v_max_u32 v128, v128, v7\n" \
  "v_cmp_eq_u32 vcc, -1, v128\n" \
  "s_and_b64 vcc, vcc, s[46:47]\n" \
  "s_cbranch_vccnz 1f\n" \
  "v_mfma_f32_32x32x16_bf16 v[96:111], v[40:43], v[4:7], v[96:111]\n" \
  "v_mfma_f32_32x32x16_bf16 v[112:127], v[44:47], v[4:7], v[112:127]\n" \
  "s_getpc_b64 s[44:45]\n" \
  "v_max3_u32 v128, v8, v9, v10\n" \
  "v_max_u32 v128, v128, v11\n" \
  "v_cmp_eq_u32 vcc, -1, v128\n" \
  "s_and_b64 vcc, vcc, s[46:47]\n" \
  "s_cbranch_vccnz 1f\n" \
  "v_mfma_f32_32x32x16_bf16 v[96:111], v[48:51], v[8:11], v[96:111]\n" \
  "v_mfma_f32_32x32x16_bf16 v[112:127], v[52:55], v[8:11], v[112:127]\n" \
  "s_getpc_b64 s[44:45]\n" \
  "v_max3_u32 v128, v12, v13, v14\n" \
  "v_max_u32 v128, v128, v15\n" \
  "v_cmp_eq_u32 vcc, -1, v128\n" \
  "s_and_b64 vcc, vcc, s[46:47]\n" \
  "s_cbranch_vccnz 1f\n" \
  "v_mfma_f32_32x32x16_bf16 v[96:111], v[56:59], v[12:15], v[96:111]\n" \
  "v_mfma_f32_32x32x16_bf16 v[112:127], v[60:63], v[12:15], v[112:127]\n" \
  "s_getpc_b64 s[44:45]\n" \
  "v_max3_u32 v128, v16, v17, v18\n" \
  "v_max_u32 v128, v128, v19\n" \
  "v_cmp_eq_u32 vcc, -1, v128\n" \
  "s_and_b64 vcc, vcc, s[46:47]\n" \
  "s_cbranch_vccnz 1f\n" \
  "v_mfma_f32_32x32x16_bf16 v[96:111], v[64:67], v[16:19], v[96:111]\n" \
  "v_mfma_f32_32x32x16_bf16 v[112:127], v[68:71], v[16:19], v[112:127]\n" \
  "s_getpc_b64 s[44:45]\n" \
  "v_max3_u32 v128, v20, v21, v22\n" \
  "v_max_u32 v128, v128, v23\n" \
  "v_cmp_eq_u32 vcc, -1, v128\n" \
  "s_and_b64 vcc, vcc, s[46:47]\n" \
  "s_cbranch_vccnz 1f\n" \
  "v_mfma_f32_32x32x16_bf16 v[96:111], v[72:75], v[20:23], v[96:111]\n" \
  "v_mfma_f32_32x32x16_bf16 v[112:127], v[76:79], v[20:23], v[112:127]\n" \
  "s_getpc_b64 s[44:45]\n" \
  "v_max3_u32 v128, v24, v25, v26\n" \
  "v_max_u32 v128, v128, v27\n" \
  "v_cmp_eq_u32 vcc, -1, v128\n" \
  "s_and_b64 vcc, vcc, s[46:47]\n" \
  "s_cbranch_vccnz 1f\n" \
  "v_mfma_f32_32x32x16_bf16 v[96:111], v[80:83], v[24:27], v[96:111]\n" \
  "v_mfma_f32_32x32x16_bf16 v[112:127], v[84:87], v[24:27], v[112:127]\n" \
  "s_getpc_b64 s[44:45]\n" \
  "v_max3_u32 v128, v28, v29, v30\n" \
  "v_max_u32 v128, v128, v31\n" \
  "v_cmp_eq_u32 vcc, -1, v128\n" \
  "s_and_b64 vcc, vcc, s[46:47]\n" \
  "s_cbranch_vccnz 1f\n" \
  "v_mfma_f32_32x32x16_bf16 v[96:111], v[88:91], v[28:31], v[96:111]\n" \
  "v_mfma_f32_32x32x16_bf16 v[112:127], v[92:95], v[28:31], v[112:127]\n" \
  "s_branch 3f\n" \
  "1:\n" \
  "s_mov_b32 s43, 0\n" \
  "v_max3_u32 v128, v0, v1, v2\n" \
  "v_max_u32 v128, v128, v3\n" \
  "v_cmp_eq_u32 vcc, -1, v128\n" \
  "s_and_b64 vcc, vcc, s[46:47]\n" \
  "s_cbranch_vccz 20f\n" \
  "buffer_load_dwordx4 v[0:3], v129, %[rs], %[ob] offen offset:0 sc1\n" \
  "s_add_u32 s43, s43, 1\n" \
  "20:\n" \
  "v_max3_u32 v128, v4, v5, v6\n" \
  "v_max_u32 v128, v128, v7\n" \
  "v_cmp_eq_u32 vcc, -1, v128\n" \
  "s_and_b64 vcc, vcc, s[46:47]\n" \
  "s_cbranch_vccz 21f\n" \
  "buffer_load_dwordx4 v[4:7], v129, %[rs], %[ob] offen offset:1024 sc1\n" \
  "s_add_u32 s43, s43, 1\n" \
  "21:\n" \
  "v_max3_u32 v128, v8, v9, v10\n" \
  "v_max_u32 v128, v128, v11\n" \
  "v_cmp_eq_u32 vcc, -1, v128\n" \
  "s_and_b64 vcc, vcc, s[46:47]\n" \
  "s_cbranch_vccz 22f\n" \
  "buffer_load_dwordx4 v[8:11], v129, %[rs], %[ob] offen offset:2048 sc1\n" \
  "s_add_u32 s43, s43, 1\n" \
  "22:\n" \
  "v_max3_u32 v128, v12, v13, v14\n" \
  "v_max_u32 v128, v128, v15\n" \
  "v_cmp_eq_u32 vcc, -1, v128\n" \
  "s_and_b64 vcc, vcc, s[46:47]\n" \
  "s_cbranch_vccz 23f\n" \
  "buffer_load_dwordx4 v[12:15], v129, %[rs], %[ob] offen offset:3072 sc1\n" \
  "s_add_u32 s43, s43, 1\n" \
  "23:\n" \
  "v_max3_u32 v128, v16, v17, v18\n" \
  "v_max_u32 v128, v128, v19\n" \
  "v_cmp_eq_u32 vcc, -1, v128\n" \
  "s_and_b64 vcc, vcc, s[46:47]\n" \
  "s_cbranch_vccz 24f\n" \
  "buffer_load_dwordx4 v[16:19], v129, %[rs], s40 offen offset:0 sc1\n" \
  "s_add_u32 s43, s43, 1\n" \
  "24:\n" \
  "v_max3_u32 v128, v20, v21, v22\n" \
  "v_max_u32 v128, v128, v23\n" \
  "v_cmp_eq_u32 vcc, -1, v128\n" \
  "s_and_b64 vcc, vcc, s[46:47]\n" \
  "s_cbranch_vccz 25f\n" \
  "buffer_load_dwordx4 v[20:23], v129, %[rs], s40 offen offset:1024 sc1\n" \
  "s_add_u32 s43, s43, 1\n" \
  "25:\n" \
  "v_max3_u32 v128, v24, v25, v26\n" \
  "v_max_u32 v128, v128, v27\n" \
  "v_cmp_eq_u32 vcc, -1, v128\n" \
  "s_and_b64 vcc, vcc, s[46:47]\n" \
  "s_cbranch_vccz 26f\n" \
  "buffer_load_dwordx4 v[24:27], v129, %[rs], s40 offen offset:2048 sc1\n" \
  "s_add_u32 s43, s43, 1\n" \
  "26:\n" \
  "v_max3_u32 v128, v28, v29, v30\n" \
  "v_max_u32 v128, v128, v31\n" \
  "v_cmp_eq_u32 vcc, -1, v128\n" \
  "s_and_b64 vcc, vcc, s[46:47]\n" \
  "s_cbranch_vccz 27f\n" \
  "buffer_load_dwordx4 v[28:31], v129, %[rs], s40 offen offset:3072 sc1\n" \
  "s_add_u32 s43, s43, 1\n" \
  "27:\n" \
  "s_sub_u32 s42, s42, 1\n" \
  "s_cmp_eq_u32 s42, 0\n" \
  "s_cselect_b64 s[46:47], 0, s[46:47]\n" \
  "s_cmp_lt_u32 s43, 4\n" \
  "s_cbranch_scc1 4f\n" \
  "s_sleep 6\n" \
  "4:\n" \
  "s_waitcnt vmcnt(0)\n" \
  "s_setpc_b64 s[44:45]\n" \
  "3:\n" \
  "s_nop 10\n" \
  "ds_write_b128 v137, v[96:99] offset:0\n" \
  "ds_write_b128 v138, v[100:103] offset:1024\n" \
  "ds_write_b128 v139, v[104:107] offset:2048\n" \
  "ds_write_b128 v140, v[108:111] offset:3072\n" \
  "ds_write_b128 v141, v[112:115] offset:4096\n" \
  "ds_write_b128 v142, v[116:119] offset:5120\n" \
  "ds_write_b128 v143, v[120:123] offset:6144\n" \
  "ds_write_b128 v144, v[124:127] offset:7168\n" \
  "s_mov_b64 exec, 1\n" \
  "s_waitcnt lgkmcnt(0)\n" \
  "ds_add_u32 v136, v133\n" \
  "s_mov_b64 exec, -1\n" \
  "8:\n" \
  "ds_read_b32 v128, v136\n" \
  "s_waitcnt lgkmcnt(0)\n" \
  "v_readfirstlane_b32 s43, v128\n" \
  "s_nop 1\n" \
  "s_cmp_ge_u32 s43, 4\n" \
  "s_cbranch_scc0 8b\n" \
  "ds_read_b128 v[0:3], v135 offset:0\n" \
  "ds_read_b128 v[4:7], v135 offset:512\n" \
  "ds_read_b128 v[8:11], v135 offset:8192\n" \
  "ds_read_b128 v[12:15], v135 offset:8704\n" \
  "ds_read_b128 v[16:19], v135 offset:16384\n" \
  "ds_read_b128 v[20:23], v135 offset:16896\n" \
  "ds_read_b128 v[24:27], v135 offset:24576\n" \
  "ds_read_b128 v[28:31], v135 offset:25088\n" \
  "s_waitcnt lgkmcnt(0)\n"
#define OUT_B \
  "ds_read_b64_tr_b16 v[144:145], v160 offset:0\n" \
  "ds_read_b64_tr_b16 v[146:147], v160 offset:512\n" \
  "ds_read_b64_tr_b16 v[148:149], v160 offset:1024\n" \
  "ds_read_b64_tr_b16 v[150:151], v160 offset:1536\n" \
  "ds_read_b64_tr_b16 v[152:153], v160 offset:2048\n" \
  "ds_read_b64_tr_b16 v[154:155], v160 offset:2560\n" \
  "ds_read_b64_tr_b16 v[156:157], v160 offset:3072\n" \
  "ds_read_b64_tr_b16 v[158:159], v160 offset:3584\n" \
  "s_waitcnt lgkmcnt(6)\n" \
  "v_mfma_f32_32x32x16_bf16 v[128:143], v[144:147], v[0:3], 0\n" \
  "ds_read_b64_tr_b16 v[144:145], v160 offset:4096\n" \
  "ds_read_b64_tr_b16 v[146:147], v160 offset:4608\n" \
  "s_waitcnt lgkmcnt(6)\n" \
  "v_mfma_f32_32x32x16_bf16 v[128:143], v[148:151], v[4:7], v[128:143]\n" \
  "ds_read_b64_tr_b16 v[148:149], v160 offset:5120\n" \
  "ds_read_b64_tr_b16 v[150:151], v160 offset:5632\n" \
  "s_waitcnt lgkmcnt(6)\n" \
  "v_mfma_f32_32x32x16_bf16 v[128:143], v[152:155], v[8:11], v[128:143]\n" \
  "ds_read_b64_tr_b16 v[152:153], v160 offset:6144\n" \
  "ds_read_b64_tr_b16 v[154:155], v160 offset:6656\n" \
  "s_waitcnt lgkmcnt(6)\n" \
  "v_mfma_f32_32x32x16_bf16 v[128:143], v[156:159], v[12:15], v[128:143]\n" \
  "ds_read_b64_tr_b16 v[156:157], v160 offset:7168\n" \
  "ds_read_b64_tr_b16 v[158:159], v160 offset:7680\n" \
  "s_waitcnt lgkmcnt(6)\n" \
  "v_mfma_f32_32x32x16_bf16 v[128:143], v[144:147], v[16:19], v[128:143]\n" \
  "ds_read_b64_tr_b16 v[144:145], v160 offset:8192\n" \
  "ds_read_b64_tr_b16 v[146:147], v160 offset:8704\n" \
  "s_waitcnt lgkmcnt(6)\n" \
  "v_mfma_f32_32x32x16_bf16 v[128:143], v[148:151], v[20:23], v[128:143]\n" \
  "ds_read_b64_tr_b16 v[148:149], v160 offset:9216\n" \
  "ds_read_b64_tr_b16 v[150:151], v160 offset:9728\n" \
  "s_waitcnt lgkmcnt(6)\n" \
  "v_mfma_f32_32x32x16_bf16 v[128:143], v[152:155], v[24:27], v[128:143]\n" \
  "ds_read_b64_tr_b16 v[152:153], v160 offset:10240\n" \
  "ds_read_b64_tr_b16 v[154:155], v160 offset:10752\n" \
  "s_waitcnt lgkmcnt(6)\n" \
  "v_mfma_f32_32x32x16_bf16 v[128:143], v[156:159], v[28:31], v[128:143]\n" \
  "ds_read_b64_tr_b16 v[156:157], v160 offset:11264\n" \
  "ds_read_b64_tr_b16 v[158:159], v160 offset:11776\n" \
  "s_waitcnt lgkmcnt(6)\n" \
  "v_mfma_f32_32x32x16_bf16 v[128:143], v[144:147], v[32:35], v[128:143]\n" \
  "ds_read_b64_tr_b16 v[144:145], v160 offset:12288\n" \
  "ds_read_b64_tr_b16 v[146:147], v160 offset:12800\n" \
  "s_waitcnt lgkmcnt(6)\n" \
  "v_mfma_f32_32x32x16_bf16 v[128:143], v[148:151], v[36:39], v[128:143]\n" \
  "ds_read_b64_tr_b16 v[148:149], v160 offset:13312\n" \
  "ds_read_b64_tr_b16 v[150:151], v160 offset:13824\n" \
  "s_waitcnt lgkmcnt(6)\n" \
  "v_mfma_f32_32x32x16_bf16 v[128:143], v[152:155], v[40:43], v[128:143]\n" \
  "ds_read_b64_tr_b16 v[152:153], v160 offset:14336\n" \
  "ds_read_b64_tr_b16 v[154:155], v160 offset:14848\n" \
  "s_waitcnt lgkmcnt(6)\n" \
  "v_mfma_f32_32x32x16_bf16 v[128:143], v[156:159], v[44:47], v[128:143]\n" \
  "ds_read_b64_tr_b16 v[156:157], v160 offset:15360\n" \
  "ds_read_b64_tr_b16 v[158:159], v160 offset:15872\n" \
  "s_waitcnt lgkmcnt(6)\n" \
  "v_mfma_f32_32x32x16_bf16 v[128:143], v[144:147], v[48:51], v[128:143]\n" \
  "ds_read_b64_tr_b16 v[144:145], v160 offset:16384\n" \
  "ds_read_b64_tr_b16 v[146:147], v160 offset:16896\n" \
  "s_waitcnt lgkmcnt(6)\n" \
  "v_mfma_f32_32x32x16_bf16 v[128:143], v[148:151], v[52:55], v[128:143]\n" \
  "ds_read_b64_tr_b16 v[148:149], v160 offset:17408\n" \
  "ds_read_b64_tr_b16 v[150:151], v160 offset:17920\n" \
  "s_waitcnt lgkmcnt(6)\n" \
  "v_mfma_f32_32x32x16_bf16 v[128:143], v[152:155], v[56:59], v[128:143]\n" \
  "ds_read_b64_tr_b16 v[152:153], v160 offset:18432\n" \
  "ds_read_b64_tr_b16 v[154:155], v160 offset:18944\n" \
  "s_waitcnt lgkmcnt(6)\n" \
  "v_mfma_f32_32x32x16_bf16 v[128:143], v[156:159], v[60:63], v[128:143]\n" \
  "ds_read_b64_tr_b16 v[156:157], v160 offset:19456\n" \
  "ds_read_b64_tr_b16 v[158:159], v160 offset:19968\n" \
  "s_waitcnt lgkmcnt(6)\n" \
  "v_mfma_f32_32x32x16_bf16 v[128:143], v[144:147], v[64:67], v[128:143]\n" \
  "ds_read_b64_tr_b16 v[144:145], v160 offset:20480\n" \
  "ds_read_b64_tr_b16 v[146:147], v160 offset:20992\n" \
  "s_waitcnt lgkmcnt(6)\n" \
  "v_mfma_f32_32x32x16_bf16 v[128:143], v[148:151], v[68:71], v[128:143]\n" \
  "ds_read_b64_tr_b16 v[148:149], v160 offset:21504\n" \
  "ds_read_b64_tr_b16 v[150:151], v160 offset:22016\n" \
  "s_waitcnt lgkmcnt(6)\n" \
  "v_mfma_f32_32x32x16_bf16 v[128:143], v[152:155], v[72:75], v[128:143]\n" \
  "ds_read_b64_tr_b16 v[152:153], v160 offset:22528\n" \
  "ds_read_b64_tr_b16 v[154:155], v160 offset:23040\n" \
  "s_waitcnt lgkmcnt(6)\n" \
  "v_mfma_f32_32x32x16_bf16 v[128:143], v[156:159], v[76:79], v[128:143]\n" \
  "ds_read_b64_tr_b16 v[156:157], v160 offset:23552\n" \
  "ds_read_b64_tr_b16 v[158:159], v160 offset:24064\n" \
  "s_waitcnt lgkmcnt(6)\n" \
  "v_mfma_f32_32x32x16_bf16 v[128:143], v[144:147], v[80:83], v[128:143]\n" \
  "ds_read_b64_tr_b16 v[144:145], v160 offset:24576\n" \
  "ds_read_b64_tr_b16 v[146:147], v160 offset:25088\n" \
  "s_waitcnt lgkmcnt(6)\n" \
  "v_mfma_f32_32x32x16_bf16 v[128:143], v[148:151], v[84:87], v[128:143]\n" \
  "ds_read_b64_tr_b16 v[148:149], v160 offset:25600\n" \
  "ds_read_b64_tr_b16 v[150:151], v160 offset:26112\n" \
  "s_waitcnt lgkmcnt(6)\n" \
  "v_mfma_f32_32x32x16_bf16 v[128:143], v[152:155], v[88:91], v[128:143]\n" \
  "ds_read_b64_tr_b16 v[152:153], v160 offset:26624\n" \
  "ds_read_b64_tr_b16 v[154:155], v160 offset:27136\n" \
  "s_waitcnt lgkmcnt(6)\n" \
  "v_mfma_f32_32x32x16_bf16 v[128:143], v[156:159], v[92:95], v[128:143]\n" \
  "ds_read_b64_tr_b16 v[156:157], v160 offset:27648\n" \
  "ds_read_b64_tr_b16 v[158:159], v160 offset:28160\n" \
  "s_waitcnt lgkmcnt(6)\n" \
  "v_mfma_f32_32x32x16_bf16 v[128:143], v[144:147], v[96:99], v[128:143]\n" \
  "ds_read_b64_tr_b16 v[144:145], v160 offset:28672\n" \
  "ds_read_b64_tr_b16 v[146:147], v160 offset:29184\n" \
  "s_waitcnt lgkmcnt(6)\n" \
  "v_mfma_f32_32x32x16_bf16 v[128:143], v[148:151], v[100:103], v[128:143]\n" \
  "ds_read_b64_tr_b16 v[148:149], v160 offset:29696\n" \
  "ds_read_b64_tr_b16 v[150:151], v160 offset:30208\n" \
  "s_waitcnt lgkmcnt(6)\n" \
  "v_mfma_f32_32x32x16_bf16 v[128:143], v[152:155], v[104:107], v[128:143]\n" \
  "ds_read_b64_tr_b16 v[152:153], v160 offset:30720\n" \
  "ds_read_b64_tr_b16 v[154:155], v160 offset:31232\n" \
  "s_waitcnt lgkmcnt(6)\n" \
  "v_mfma_f32_32x32x16_bf16 v[128:143], v[156:159], v[108:111], v[128:143]\n" \
  "ds_read_b64_tr_b16 v[156:157], v160 offset:31744\n" \
  "ds_read_b64_tr_b16 v[158:159], v160 offset:32256\n" \
  "s_waitcnt lgkmcnt(6)\n" \
  "v_mfma_f32_32x32x16_bf16 v[128:143], v[144:147], v[112:115], v[128:143]\n" \
  "s_waitcnt lgkmcnt(4)\n" \
  "v_mfma_f32_32x32x16_bf16 v[128:143], v[148:151], v[116:119], v[128:143]\n" \
  "s_waitcnt lgkmcnt(2)\n" \
  "v_mfma_f32_32x32x16_bf16 v[128:143], v[152:155], v[120:123], v[128:143]\n" \
  "s_waitcnt lgkmcnt(0)\n" \
  "v_mfma_f32_32x32x16_bf16 v[128:143], v[156:159], v[124:127], v[128:143]\n" \
  "s_nop 15\n"


DI unsigned rfl(unsigned x) { return __builtin_amdgcn_readfirstlane(x); }
DI unsigned cvtpk(float lo, float hi) { f32x2 v = {lo, hi}; bf16x2 b = __builtin_convertvector(v, bf16x2); return __builtin_bit_cast(unsigned, b); }
DI f32x16 mfma32(u32x4 a, u32x4 b, f32x16 c) { return __builtin_amdgcn_mfma_f32_32x32x16_bf16(__builtin_bit_cast(bf16x8, a), __builtin_bit_cast(bf16x8, b), c, 0, 0, 0); }
DI u32x4 ldsread(unsigned addr) { return *(LAS u32x4*)addr; }
DI void ldswrite(unsigned addr, u32x4 v) { *(LAS u32x4*)addr = v; }
template <int OFF> DI void glds(const void* sbase, unsigned voff, unsigned ldst) {
  asm volatile("s_mov_b32 m0, %2\n\ts_nop 0\n\tglobal_load_lds_dwordx4 %0, %1 offset:%c3" :: "v"(voff), "s"(sbase), "s"(ldst - OFF), "i"(OFF) : "memory");
}
template <int OFF> DI void glds_dev(const void* sbase, unsigned voff, unsigned ldst) {
  asm volatile("s_mov_b32 m0, %2\n\ts_nop 0\n\tglobal_load_lds_dwordx4 %0, %1 offset:%c3 sc1" :: "v"(voff), "s"(sbase), "s"(ldst - OFF), "i"(OFF) : "memory");
}
#define WAITV(n) asm volatile("s_waitcnt vmcnt(" #n ")" ::: "memory")
#define HWBAR() asm volatile("s_waitcnt lgkmcnt(0)\n\ts_barrier" ::: "memory")
#define RLX_AGENT __ATOMIC_RELAXED, __HIP_MEMORY_SCOPE_AGENT
#define RLX_WG __ATOMIC_RELAXED, __HIP_MEMORY_SCOPE_WORKGROUP
DI u32x4 pack8(const f32x16& v, int o, float sc) {
  return (u32x4){cvtpk(v[o] * sc, v[o + 1] * sc), cvtpk(v[o + 2] * sc, v[o + 3] * sc), cvtpk(v[o + 4] * sc, v[o + 5] * sc), cvtpk(v[o + 6] * sc, v[o + 7] * sc)};
}
DI u32x4 pack8(const f32x16& v, int o) {
  return (u32x4){cvtpk(v[o], v[o + 1]), cvtpk(v[o + 2], v[o + 3]), cvtpk(v[o + 4], v[o + 5]), cvtpk(v[o + 6], v[o + 7])};
}
typedef __amdgpu_buffer_rsrc_t rsrc_t;
DI u32x4 ld_dev(rsrc_t rs, unsigned voff, unsigned soff) { return __builtin_amdgcn_raw_buffer_load_b128(rs, voff, soff, 16); }
DI void st_dev(rsrc_t rs, u32x4 v, unsigned voff, unsigned soff) {
  __builtin_amdgcn_raw_buffer_store_b128(v, rs, voff, soff, 16);
  asm volatile("s_nop 1" :: "v"(v) : "memory");
}
DI void poll32(char* ws, unsigned off, u64 tag, int l) {
  GAS u64* p = (GAS u64*)(ws + off) + (l & 31);
#pragma unroll 1
  for (int n = 0; n < 400000; ++n) {
    const u64 v = __hip_atomic_load(p, RLX_AGENT);
    if (__all(v == tag)) break;
    __builtin_amdgcn_s_sleep(8);
  }
  asm volatile("" ::: "memory");
}
DI void flag_set(char* ws, unsigned off, u64 tag) {
  asm volatile("s_mov_b64 exec, 1\n\tglobal_store_dwordx2 %0, %1, %2 sc1\n\ts_mov_b64 exec, -1" :: "v"(off), "v"(tag), "s"(ws) : "memory");
}
struct Ctl { unsigned a, one; };
DI Ctl ctl_make(unsigned base) { Ctl c = {base, 1u}; asm volatile("" : "+v"(c.a), "+v"(c.one)); return c; }
template <unsigned OFF, int SLEEP> DI void lds_wait(const Ctl& c, unsigned want) {
  while (rfl(__hip_atomic_load((LAS unsigned*)(c.a + OFF), RLX_WG)) < want) { if (SLEEP) __builtin_amdgcn_s_sleep(SLEEP); }
  asm volatile("" ::: "memory");
}
template <unsigned OFF> DI unsigned lds_inc(const Ctl& c) {
  unsigned old;
  asm volatile("s_mov_b64 exec, 1\n\ts_waitcnt lgkmcnt(0)\n\tds_add_rtn_u32 %0, %1, %2 offset:%c3\n\ts_mov_b64 exec, -1\n\ts_waitcnt lgkmcnt(0)" : "=&v"(old) : "v"(c.a), "v"(c.one), "i"(OFF) : "memory");
  return rfl(old);
}
template <unsigned OFF> DI void lds_add(const Ctl& c) {
  asm volatile("s_mov_b64 exec, 1\n\ts_waitcnt lgkmcnt(0)\n\tds_add_u32 %0, %1 offset:%c2\n\ts_mov_b64 exec, -1" :: "v"(c.a), "v"(c.one), "i"(OFF) : "memory");
}
DI f32x4 mfma16(u32x4 a, u32x4 b) { return __builtin_amdgcn_mfma_f32_16x16x32_bf16(__builtin_bit_cast(bf16x8, a), __builtin_bit_cast(bf16x8, b), (f32x4){}, 0, 0, 0); }
DI u32x4 tokpack(u32x4 v) {
  auto a = __builtin_amdgcn_permlane16_swap(v[0], v[2], false, false);
  auto b = __builtin_amdgcn_permlane16_swap(v[1], v[3], false, false);
  return (u32x4){a[0], b[0], a[1], b[1]};
}
DI int lane_now() {
  int v;
  asm volatile("v_mbcnt_lo_u32_b32 %0, -1, 0\n\tv_mbcnt_hi_u32_b32 %0, -1, %0" : "=v"(v));
  __builtin_assume(v >= 0 && v < 64);
  return v;
}
DI void phi16(f32x16& a) {
#pragma unroll
  for (int i = 0; i < 16; i += 2) {
    const f32x2 t = {a[i], a[i + 1]}, u = t * LOG2E, w = t + (1.0f + 1e-6f);
    f32x2 e = {__builtin_amdgcn_fmed3f(__builtin_amdgcn_exp2f(u[0]), 0.f, 1.f), __builtin_amdgcn_fmed3f(__builtin_amdgcn_exp2f(u[1]), 0.f, 1.f)};
    e += 1e-6f;
    a[i] = __builtin_amdgcn_fmed3f(w[0], e[0], INFINITY); a[i + 1] = __builtin_amdgcn_fmed3f(w[1], e[1], INFINITY);
  }
}
DI unsigned attn_ones(int r) { return ((r == 16) || (r == 20)) ? 0x3f803f80u : 0u; }
DI float xhalf_max(float m) { auto t = __builtin_amdgcn_permlane32_swap(__float_as_uint(m), __float_as_uint(m), false, false); return fmaxf(__uint_as_float(t[0]), __uint_as_float(t[1])); }
DI u32x4 rowpack(u32x4 v) {
  auto a = __builtin_amdgcn_permlane32_swap(v[0], v[2], false, false);
  auto b = __builtin_amdgcn_permlane32_swap(v[1], v[3], false, false);
  return (u32x4){a[0], b[0], a[1], b[1]};
}
DI f32x16 smf(u32x4 v, const f32x16& p, int o, f32x16 acc) {
  f32x8 h = {p[o], p[o + 1], p[o + 2], p[o + 3], p[o + 4], p[o + 5], p[o + 6], p[o + 7]};
  return __builtin_amdgcn_smfmac_f32_32x32x32_bf16(__builtin_bit_cast(bf16x8, v), __builtin_bit_cast(bf16x16, h), acc, (int)0xdddddddd, 0, 0);
}


#define CL8(a) "v" #a "0","v" #a "1","v" #a "2","v" #a "3","v" #a "4","v" #a "5","v" #a "6","v" #a "7","v" #a "8","v" #a "9"
#define CL_PROJ "v48","v49",CL8(5),CL8(6),CL8(7),CL8(8),CL8(9),CL8(10),"v110","v111","v124","v125","v126","v127","v128","v129",CL8(13),"v140","v141","v142","v143","v144","v145","v146","v147","s40","s41","s42","s43","s44","s45","s46","s47","s48","s49","s50","s51","scc","memory"
#define CL_ATTN "v0","v1","v2","v3","v4","v5","v6","v7","v8","v9",CL8(1),"v20","v21","v22","v23","v64","v65","v66","v67","v68","v69",CL8(7),CL8(8),"v90","v91","v92","v93","v94","v95","v104","v105","v106","v107","v108","v109","v110","v111","s40","s41","s42","s43","s44","vcc","scc","memory"
#define CL_OUT "v32","v33","v34","v35","v36","v37","v38","v39",CL8(4),CL8(5),CL8(6),CL8(7),CL8(8),CL8(9),CL8(10),CL8(11),"v120","v121","v122","v123","v124","v125","v126","v127","v128","v133","v137","v138","v139","v140","v141","v142","v143","v144","s40","s42","s43","s44","s45","s46","s47","vcc","scc","memory"
#define BAR() asm volatile("s_barrier" ::: "memory")

__global__ __launch_bounds__(768, 3) void fused_kernel(const char* __restrict__ xa, const char* __restrict__ wa, const char* __restrict__ woa,
                                                       const char* __restrict__ xb, const char* __restrict__ wb, const char* __restrict__ wob,
                                                       char* __restrict__ ws, char* __restrict__ out) {
  extern __shared__ __attribute__((aligned(16))) char lds[];
  const unsigned lb = (unsigned)(size_t)(LAS char*)lds;
  const int wv = rfl(threadIdx.x >> 6);
  const int bx = blockIdx.x & 7, bi = blockIdx.x >> 3;
  const int pt = 8 * (bx >> 1) + (bi & 7), pg = 4 * (bx & 1) + (bi >> 3);
  const int ah = 4 * bx + (bi & 3), atb = bi >> 2;
  const int orb = 4 * bx + (bi >> 3), ocb = bi & 7;
  const int wj = wv & 3, role = wv >> 2;
  const rsrc_t rs = __builtin_amdgcn_make_buffer_rsrc(ws, 0, WS_BYTES, 0x00020000);
  const char* pc0;
  asm volatile("s_getpc_b64 %0" : "=s"(pc0));
  pc0 = (const char*)((size_t)pc0 & ~(size_t)255);
#define LANE() const int l = lane_now(), r = l & 31, hh = l >> 5, g = l >> 4, q = (l & 15) >> 2, pp = l & 3; (void)r; (void)hh; (void)g; (void)q; (void)pp
  if (__builtin_expect(role == 0, 1)) {
    LANE();
    const int rot = (2 * (bx >> 1) + 4 * (bx & 1)) & 7;
    const unsigned x0 = (pt * 64 + wj * 16 + (l >> 3)) * 1024 + (((l & 7) ^ (l >> 4)) << 4), x1 = (x0 ^ 64) + 8192;
    const unsigned w0 = (wj * 16 + (l >> 3)) * 3072 + pg * 128 + (((l & 7) ^ (((l >> 4) & 1) << 1)) << 4), w1 = (w0 ^ 64) + 8 * 3072;
    asm volatile(LOADER :: [x0] "v"(x0), [x1] "v"(x1), [w0] "v"(w0), [w1] "v"(w1), [xa] "s"(xa), [wa] "s"(wa), [xb] "s"(xb), [wb] "s"(wb), [rot] "s"(rot), [d] "s"(lb + wj * 2048)
                 : "v0","v1","v2","v3","s40","s41","s42","s43","s44","s45","s46","s47","s48","s49","scc","memory");
  }
  asm volatile("" :: "s"(xa), "s"(wa), "s"(woa), "s"(xb), "s"(wb), "s"(wob), "s"(ws), "s"(out));
  auto kvload = [&](int c, int l) {
    const char* kp = ws + WS_K + (ah * 64 + c * 8 + 2 * wj) * 1024; const char* vp = kp + (WS_V - WS_K);
    const unsigned d = lb + c * 16384 + wj * 2048;
    glds_dev<0>(kp, l * 16, d); glds_dev<1024>(kp, l * 16, d + 1024); glds_dev<0>(vp, l * 16, d + 8192); glds_dev<1024>(vp, l * 16, d + 9216);
  };

  const int kh = role;
  f32x16 oa, ob, vfr; f32x8 sbh; unsigned idx, one;
#define ATTN_SETUP() asm volatile(ATTN_INIT : "={v[32:47]}"(oa), "={v[48:63]}"(ob), "={v[112:127]}"(vfr), "={v[24:31]}"(sbh), "={v128}"(idx), "={v132}"(one) : "{v131}"(attn_ones(r)))
  const unsigned qo = WS_Q + (ah * 64 + atb * 8 + 2 * wj) * 1024;
  if (role == 0) {
    LANE();
    WAITV(8); BAR();
    WAITV(0); BAR();
    ATTN_SETUP();
    BAR();
  } else if (role == 1) {
    LANE();
    u64 tag = __hip_atomic_load((GAS u64*)(ws + WS_EPOCH), RLX_AGENT) + 1;
    if (wj == 1) *(LAS unsigned*)(lb + L_CTL + l * 4) = 0u;
    __builtin_amdgcn_s_setprio(2);
    if (wj == 3) {
      if (bi < CODE_KB) glds<0>(pc0, bi * 1024 + l * 16, lb + L_RED + 7168);
    }
    auto pf_bad = [&](const char* wsrc) {
      const unsigned bad = (3u - (unsigned)((size_t)wsrc >> 7)) & 7u;
      if ((bad >> 2) == (unsigned)(bx & 1) && wj < 3) {
        const unsigned o = (bi + 32 * (l >> 3)) * 3072 + bad * 128 + wj * 1024 + (l & 7) * 16;
        glds<0>(wsrc, o, lb + L_EX + 16384 + wj * 1024); glds<0>(wsrc, o + 256 * 3072, lb + L_EX + 16384 + wj * 1024);
      }
    };
    __builtin_amdgcn_s_sleep(2);
    pf_bad(wa);
    const unsigned u = blockIdx.x * 4 + wj, jk = lb + L_EX + 20480 + wj * 1024;
    auto pf_b = [&]() {
      glds<0>(xb, u * 2048 + l * 16, jk); glds<1024>(xb, u * 2048 + l * 16, jk + 1024);
      glds<0>(wb, u * 1536 + l * 16, jk); glds<512>(wb, u * 1536 + l * 16, jk + 512);
    };
    HWBAR();
#pragma unroll 1
    for (int s = 0; s < 13 - 1; ++s) {
      if (s == 2) pf_b();
      if (s == 4) pf_bad(wb);
      BAR();
    }
    tag = ((u64)rfl((unsigned)(tag >> 32)) << 32) | rfl((unsigned)tag);
    ATTN_SETUP();
    int state = 0;
    const unsigned pb = lb + L_CTL + 256 + wj * 128;
    const char* fp = ws + WS_FA + ((bx * 2 + ((ah >> 1) & 1)) * 4 + wj) * 128;
#pragma unroll 1
    for (int s = 13; s < 400000; ++s) {
      if (s <= 16) BAR();
      if (state < 2 && (s > 16 || (__builtin_amdgcn_s_getreg(7 | (3 << 11)) & 15) == 0)) {
        WAITV(0);
        if (state == 1 && __all(*(LAS u64*)(pb + (l & 15) * 8) == tag)) {
          state = 2;
          glds_dev<0>(ws + qo, l * 16, lb + L_EX + wj * 2048); glds_dev<1024>(ws + qo, l * 16, lb + L_EX + wj * 2048 + 1024);
          kvload(0, l); kvload(1, l);
          lds_add<C_SEEN>(ctl_make(lb + L_CTL + 16 * wj));
        } else {
          state = 1;
          asm volatile("s_mov_b32 m0, %2\n\ts_mov_b64 exec, 0xff\n\tglobal_load_lds_dwordx4 %0, %1 sc1\n\ts_mov_b64 exec, -1" :: "v"(l * 16), "s"(fp), "s"(pb) : "memory");
        }
      }
      if (state == 2 && s >= 16) break;
    }
    const Ctl ctl = ctl_make(lb + L_CTL);
    if (state < 2) lds_add<C_SEEN>(ctl_make(lb + L_CTL + 16 * wj));
    WAITV(0);
    lds_add<C_RDY>(ctl);
  } else {
    LANE();
    __builtin_amdgcn_s_setprio(3);
    u64 tag;
    const int jp = wj & 1, jt = wj >> 1;
    f32x16 aq, ak, av;
    const unsigned fo = ((4 * pg + 2 * jp) * 64 + 2 * pt + jt) * 1024;
    {
      const unsigned xr = lb + jt * 4096 + (l & 15) * 128, xz = (l >> 1) & 7;
      const unsigned wa0 = lb + 8192 + (8 * g + q) * 128 + (((2 * jp) ^ ((q >> 1) | ((g & 1) << 1))) << 5) + pp * 8;
      u32x4 ad = {xr + ((g ^ xz) << 4), xr + (((4 + g) ^ xz) << 4), wa0, wa0 ^ 32};
      const unsigned ko = (hh * 32 + ((r & 3) | ((r & 12) << 1) | ((r & 16) >> 2))) * 16;
      const unsigned fl = WS_FA + (((pg * 2 + jp) * 4 + (pt & 3)) * 16 + (pt >> 2) * 2 + jt) * 8;
      asm volatile(PROJ : "={v[0:15]}"(aq), "={v[16:31]}"(ak), "={v[32:47]}"(av), "+{v[112:115]}"(ad), "={v[120:121]}"(tag)
                   : "{v117}"(ko), "{v118}"(l * 16), "{v119}"(l * 16), "{v122}"(fl), [rs] "s"(rs), [fo] "s"(fo), [ws] "s"(ws) : CL_PROJ);
    }
    phi16(ak); phi16(aq);
    asm volatile("" : "+v"(ak), "+v"(aq));
    lds_wait<C_SEEN, 0>(ctl_make(lb + L_CTL + 16 * wj), 1);
    const Ctl ctl = ctl_make(lb + L_CTL);
#pragma unroll 1
    for (int c = 2; c < 8; ++c) kvload(c, l);
    tag = ((u64)rfl((unsigned)(tag >> 32)) << 32) | rfl((unsigned)tag);
    {
      const u32x4 k0 = pack8(ak, 0), k1 = pack8(ak, 8);
      const u32x4 one = {0x3f803f80u, 0x3f803f80u, 0x3f803f80u, 0x3f803f80u};
      const f32x4 kv0 = mfma16(k0, pack8(av, 0)), z0 = mfma16(k0, one), kv1 = mfma16(k1, pack8(av, 8)), z1 = mfma16(k1, one);
      st_dev(rs, tokpack(pack8(aq, 0)), l * 16, WS_PQ + fo); st_dev(rs, tokpack(pack8(aq, 8)), l * 16, WS_PQ + fo + 65536);
      WAITV(2);
      lds_add<C_ALL>(ctl);
      lds_wait<C_QRD, 2>(ctl, 8);
      unsigned e = lb + L_RED + l * 16;
      asm volatile("" : "+v"(e));
      auto swap_sum = [&](f32x4 gkv, f32x4 gz, f32x4 kv, f32x4 z) {
        ldswrite(e + wj * 2048, __builtin_bit_cast(u32x4, gkv)); ldswrite(e + wj * 2048 + 1024, __builtin_bit_cast(u32x4, gz));
        lds_add<C_X>(ctl);
        lds_wait<C_X, 2>(ctl, 4);
        kv += __builtin_bit_cast(f32x4, ldsread(e + (wj ^ 2) * 2048)); z += __builtin_bit_cast(f32x4, ldsread(e + (wj ^ 2) * 2048 + 1024));
        const unsigned vo = hh * (17 * 32) + (g & 1) * 16, hb = WS_KVP + ((4 * pg + 2 * jp + jt) * 32 + pt) * KVP_STRIDE;
        st_dev(rs, __builtin_bit_cast(u32x4, kv), vo + (l & 15) * 32, hb);
        if ((l & 15) == 0) st_dev(rs, __builtin_bit_cast(u32x4, z), vo + 512, hb);
      };
      if (jt) swap_sum(kv0, z0, kv1, z1); else swap_sum(kv1, z1, kv0, z0);
      WAITV(0);
      if (lds_inc<C_FB>(ctl) == 3) flag_set(ws, WS_FB + (pg * 32 + pt) * 8, tag);
    }
    {
      const unsigned t0 = atb * 8 + 2 * wj;
      const u32x4 poison = {POISON, POISON, POISON, POISON};
      st_dev(rs, poison, l * 16, WS_OA + (t0 * 32 + ah) * 1024);
      st_dev(rs, poison, l * 16, WS_OA + ((t0 + 1) * 32 + ah) * 1024);
      {
        const unsigned u = blockIdx.x * 4 + wj;
        const u32x4 junk = *(const u32x4*)(((u & 1) ? wob : woa) + (u >> 1) * 1024 + l * 16);
        asm volatile("" :: "v"(junk));
      }
      if (wj == 0) { poll32(ws, WS_FB + bx * 256, tag, l); lds_add<C_PB>(ctl); } else lds_wait<C_PB, 8>(ctl, 1);
      const unsigned vo = (hh * 17 + (r < 16 ? r : 16)) * 32;
      const unsigned hb = WS_KVP + (ah * 32 + wj * 8) * KVP_STRIDE;
      u32x4 pa[8], pb[8];
#pragma unroll
      for (int i = 0; i < 8; ++i) { pa[i] = ld_dev(rs, vo, hb + i * KVP_STRIDE); pb[i] = ld_dev(rs, vo + 16, hb + i * KVP_STRIDE); }
      const u32x4 pq0 = ld_dev(rs, l * 16, WS_PQ + (ah * 64 + t0) * 1024), pq1 = ld_dev(rs, l * 16, WS_PQ + (ah * 64 + t0 + 1) * 1024);
      f32x4 s0 = __builtin_bit_cast(f32x4, pa[0]), s1 = __builtin_bit_cast(f32x4, pb[0]);
#pragma unroll
      for (int i = 1; i < 8; ++i) { s0 += __builtin_bit_cast(f32x4, pa[i]); s1 += __builtin_bit_cast(f32x4, pb[i]); }
      lds_wait<C_FB, 2>(ctl, 4);
      unsigned rd = lb + L_RED + l * 16;
      asm volatile("" : "+v"(rd));
      ldswrite(rd + wj * 2048, __builtin_bit_cast(u32x4, s0)); ldswrite(rd + wj * 2048 + 1024, __builtin_bit_cast(u32x4, s1));
      lds_add<C_RED>(ctl);
      lds_wait<C_RED, 2>(ctl, 4);
      s0 = __builtin_bit_cast(f32x4, ldsread(rd)); s1 = __builtin_bit_cast(f32x4, ldsread(rd + 1024));
#pragma unroll
      for (int i = 1; i < 4; ++i) { s0 += __builtin_bit_cast(f32x4, ldsread(rd + i * 2048)); s1 += __builtin_bit_cast(f32x4, ldsread(rd + i * 2048 + 1024)); }
      const u32x4 af = {cvtpk(s0[0], s0[1]), cvtpk(s0[2], s0[3]), cvtpk(s1[0], s1[1]), cvtpk(s1[2], s1[3])};
      const f32x16 n0 = mfma32(af, pq0, (f32x16){}), n1 = mfma32(af, pq1, (f32x16){});
      st_dev(rs, pack8(n0, 0, __builtin_amdgcn_rcpf(n0[8] + 1e-6f)), l * 16, WS_OB + (t0 * 32 + ah) * 1024);
      st_dev(rs, pack8(n1, 0, __builtin_amdgcn_rcpf(n1[8] + 1e-6f)), l * 16, WS_OB + ((t0 + 1) * 32 + ah) * 1024);
      WAITV(0);
      if (lds_inc<C_OB>(ctl) == 3) flag_set(ws, WS_FOB + (atb * 32 + ah) * 8, tag);
    }
    lds_wait<C_P3, 8>(ctl, 8);
    {
      const unsigned src = (wj * 128 + (l >> 3)) * 1024 + ocb * 128 + (((l & 7) ^ (((l >> 4) & 1) << 2)) << 4);
#pragma unroll 1
      for (int i = 0; i < 16; ++i) glds<0>(woa + i * 8192, src, lb + (wj * 16 + i) * 1024);
    }
    if (wj == 0) {
      poll32(ws, WS_FOB + bx * 256, tag, l);
      lds_add<C_POB>(ctl);
      if (blockIdx.x == 0) flag_set(ws, WS_EPOCH, tag);
    } else lds_wait<C_POB, 8>(ctl, 1);
    const int brt = bx * 8 + (bi >> 4) * 4 + wj, bcb = bi & 15;
    u32x32 of[4];
#pragma unroll
    for (int i = 0; i < 32; ++i) {
      const u32x4 t = ld_dev(rs, l * 16, WS_OB + (brt * 32 + i) * 1024);
#pragma unroll
      for (int k = 0; k < 4; ++k) of[i >> 3][(i & 7) * 4 + k] = t[k];
    }
    lds_wait<C_P5, 8>(ctl, 8);
    {
      const unsigned src = (wj * 128 + (l >> 2)) * 1024 + bcb * 64 + (l & 3) * 16;
#pragma unroll 1
      for (int i = 0; i < 8; ++i) glds<0>(wob + i * 16384, src, lb + 65536 + (wj * 8 + i) * 1024);
    }
    WAITV(0);
    __builtin_amdgcn_s_setprio(0);
    HWBAR();
    lds_wait<C_WA, 2>(ctl, 8);
    f32x16 o;
    asm volatile(OUT_B : "={v[128:143]}"(o) : "{v[0:31]}"(of[0]), "{v[32:63]}"(of[1]), "{v[64:95]}"(of[2]), "{v[96:127]}"(of[3]),
                 "{v160}"(lb + 65536 + (4 * (g >> 1) + q) * 64 + (g & 1) * 32 + pp * 8)
                 : "v144","v145","v146","v147","v148","v149",CL8(15),"memory");
    const int l2 = lane_now();
    char* dst = out + (size_t)(brt * 32 + (l2 & 31)) * 2048 + 1024 + bcb * 64 + (l2 >> 5) * 16;
    *(u32x4*)dst = rowpack(pack8(o, 0));
    *(u32x4*)(dst + 32) = rowpack(pack8(o, 8));
    return;
  }
  LANE();

  lds_wait<C_RDY, 0>(ctl_make(lb + L_CTL), 4);
  u32x4 qa, qb;
  unsigned bad;
  const unsigned oo = WS_OA + ((atb * 8 + 2 * wj + kh) * 32 + ah) * 1024;
  {
    unsigned kad = lb + kh * 4096 + l * 16, vad = lb + 8192 + kh * 4096 + hh * 256 + (l & 15) * 16, k0a = lb + hh * 512;
    const unsigned e = lb + L_EX + 8192 + wj * 2304;
    asm volatile(ATTN_LOOP : "+{v[32:47]}"(oa), "+{v[48:63]}"(ob), "+{v[112:127]}"(vfr), "+{v[24:31]}"(sbh), "+{v129}"(kad), "+{v130}"(vad), "+{v133}"(k0a),
                   "={v[96:99]}"(qa), "={v[100:103]}"(qb), [bad] "=&s"(bad)
                 : "{v128}"(idx), "{v132}"(one), "{v134}"(lb + L_CTL + C_P3), "{v135}"(e + l * 16), "{v136}"(e + 2048 + l * 4), "{v137}"(lb + L_EX + wj * 2048 + l * 16),
                   "{v138}"(l * 16), [kh] "s"(kh), [rs] "s"(rs), [oo] "s"(oo) : CL_ATTN);
  }
  if (__builtin_expect(bad, 0)) {
    if (kh) qa = qb;
    const unsigned ones = attn_ones(r);
    float mx = -INFINITY;
#pragma unroll 1
    for (int t = 0; t < 64; ++t) {
      const f32x16 sc = mfma32(ld_dev(rs, l * 16, WS_K + (ah * 64 + t) * 1024), qa, (f32x16){});
#pragma unroll
      for (int i = 0; i < 16; ++i) mx = fmaxf(mx, sc[i]);
    }
    mx = xhalf_max(mx);
    f32x16 o = {};
#pragma unroll 1
    for (int t = 0; t < 64; ++t) {
      const unsigned to = (ah * 64 + t) * 1024;
      f32x16 sc = mfma32(ld_dev(rs, l * 16, WS_K + to), qa, (f32x16){});
      u32x4 v0 = ld_dev(rs, hh * 256 + (l & 15) * 16, WS_V + to), v1 = ld_dev(rs, hh * 256 + (l & 15) * 16 + 512, WS_V + to);
      if (r & 16) { v0 = (u32x4){ones, ones, ones, ones}; v1 = v0; }
#pragma unroll
      for (int i = 0; i < 16; ++i) sc[i] = __builtin_amdgcn_exp2f(sc[i] - mx);
      o = smf(v0, sc, 0, o); o = smf(v1, sc, 8, o);
    }
    st_dev(rs, pack8(o, 0, __builtin_amdgcn_rcpf(o[8])), l * 16, oo);
  }
  __builtin_amdgcn_s_setprio(1);
  {
    const int rt = wj & 1, kq = (wj >> 1) + 2 * kh;
    const unsigned ob_ = WS_OA + ((orb * 2 + rt) * 32 + kq * 8) * 1024;
    const unsigned w0 = lb + kq * 16384 + (4 * (g >> 1) + q) * 128 + ((q >> 1) << 6) + (g & 1) * 32 + pp * 8;
    char* dst = out + (size_t)(orb * 64 + rt * 32 + 8 * kq + (l >> 3)) * 2048 + ocb * 128 + (l & 7) * 16;
    asm volatile("" : "+v"(dst));
    f32x16 pa, pb;
    asm volatile(OUT_A : "={v[0:15]}"(pa), "={v[16:31]}"(pb)
                 : "{v129}"(l * 16), "{v130}"(w0), "{v131}"(w0 ^ 64), "{v132}"(lb + L_CTL + C_WA), "{v134}"(lb + (rt * 4 + kq) * 8192),
                   "{v135}"(lb + rt * 32768 + (l & 7) * 1024 + (((8 * kq + (l >> 3)) ^ (2 * (l & 7))) * 16)), "{v136}"(lb + L_CTL + C_S3 + 4 * rt), [rs] "s"(rs), [ob] "s"(ob_) : CL_OUT);
    f32x4 f0, f1;
#pragma unroll
    for (int i = 0; i < 4; ++i) { f0[i] = ((pa[i] + pa[8 + i]) + pb[i]) + pb[8 + i]; f1[i] = ((pa[4 + i] + pa[12 + i]) + pb[4 + i]) + pb[12 + i]; }
    const u32x4 res = {cvtpk(f0[0], f0[1]), cvtpk(f0[2], f0[3]), cvtpk(f1[0], f1[1]), cvtpk(f1[2], f1[3])};
    asm volatile("global_store_dwordx4 %0, %1, off nt\n\ts_nop 1" :: "v"(dst), "v"(res) : "memory");
  }
}

extern "C" void kernel_launch(void* const* d_in, const int* in_sizes, int n_in, void* d_out, int out_size, void* d_ws, size_t ws_size, hipStream_t stream) {
  (void)hipFuncSetAttribute((const void*)fused_kernel, hipFuncAttributeMaxDynamicSharedMemorySize, LDS_BYTES);
  fused_kernel<<<256, 768, LDS_BYTES, stream>>>((const char*)d_in[0], (const char*)d_in[1], (const char*)d_in[2], (const char*)d_in[3], (const char*)d_in[4], (const char*)d_in[5],
                                                (char*)d_ws, (char*)d_out);
}
```
